```python
import torch

def _softmax(x, axis=-1):
    return torch.softmax(x, dim=axis)
def _gelu(x):
    return torch.nn.functional.gelu(x, approximate="tanh")
def _silu(x):
    return torch.nn.functional.silu(x)
def _sigmoid(x):
    return torch.sigmoid(x)
def _layernorm(x, axis=-1, eps=1e-5):
    m = x.mean(dim=axis, keepdim=True)
    v = x.var(dim=axis, keepdim=True, unbiased=False)
    return (x - m) / torch.sqrt(v + eps)
def _scatter_add(tgt, idx, src, axis=0):
    return tgt.clone().index_add_(axis, idx, src)
def _segment_sum(src, seg, n):
    out = torch.zeros((n,) + src.shape[1:], dtype=src.dtype)
    return out.index_add_(0, seg, src)
def _pad(x, pw):
    flat = tuple(v for pair in reversed(pw) for v in pair)
    return torch.nn.functional.pad(x, flat)

kernel_name = 'gated_mix__sliding_block_cached__swiglu_ffn__decode'

def setup_inputs():

    _w = (lambda a: (lambda t: t.to(torch.bfloat16) if t.is_floating_point() else t)(torch.as_tensor(a)))
    def _A_setup():
        B, Sq, D = 8, 1, 576
        Hq, Hkv, Sk, Dh = 32, 16, 4096, 128
        assert Hq % Hkv == 0, (Hq, Hkv)
        return {
            "x": _w((lambda *s: torch.randn(*s, dtype=torch.bfloat16) * 0.05)(B, Sq, D)),
            "k_cache": _w((lambda *s: torch.randn(*s, dtype=torch.bfloat16) * 3.2)(B, Hkv, Sk, Dh)),
            "v_cache": _w((lambda *s: torch.randn(*s, dtype=torch.bfloat16) * 0.4)(B, Hkv, Sk, Dh)),
            "wq": _w((lambda *s: torch.randn(*s, dtype=torch.bfloat16) * 3.2)(D, Hq * Dh) * (D ** -0.5)),
            "wo": _w((lambda *s: torch.randn(*s, dtype=torch.bfloat16) * 0.4)(Hq * Dh, D) * ((Hq * Dh) ** -0.5)),
        }
    _A_in = _A_setup()

    def _B_setup():
        B, S, D, F = 8, 1, 576, 4608
        return {
            "x": _w((lambda *s: torch.randn(*s, dtype=torch.bfloat16) * 0.05)(B, S, D)),
            "w_gate": _w((lambda *s: torch.randn(*s, dtype=torch.bfloat16) * 0.05)(D, F)),
            "w_up": _w((lambda *s: torch.randn(*s, dtype=torch.bfloat16) * 0.05)(D, F)),
            "w_down": _w((lambda *s: torch.randn(*s, dtype=torch.bfloat16) * 0.05)(F, D)),
        }
    _B_in = _B_setup()

    return {
        "A_x": _A_in["x"],
        "A_k_cache": _A_in["k_cache"],
        "A_v_cache": _A_in["v_cache"],
        "A_wq": _A_in["wq"],
        "A_wo": _A_in["wo"],
        "B_x": _B_in["x"],
        "B_w_gate": _B_in["w_gate"],
        "B_w_up": _B_in["w_up"],
        "B_w_down": _B_in["w_down"],
        "gate_w": _w((lambda *s: torch.randn(*s, dtype=torch.bfloat16) * 0.05)(576) * 16.0),
    }

def reference(A_x, A_k_cache, A_v_cache, A_wq, A_wo, B_x, B_w_gate, B_w_up, B_w_down, gate_w):
    def _A_ref(x, k_cache, v_cache, wq, wo):
        B, Sq, D = x.shape
        Hq, Dh = 32, 128
        Hkv, Sk = k_cache.shape[1], k_cache.shape[2]
        W = min(4096, max(256, Sk // 4))
        rep = Hq // Hkv
        q = (lambda x, *d: x.permute(*d))((x @ wq).reshape(B, Sq, Hq, Dh), 0, 2, 1, 3)
        k = (lambda x, n, axis: torch.repeat_interleave(x, n, dim=axis))(k_cache[:, :, Sk - W:, :], rep, axis=1)
        v = (lambda x, n, axis: torch.repeat_interleave(x, n, dim=axis))(v_cache[:, :, Sk - W:, :], rep, axis=1)
        scores = torch.einsum("bhqd,bhkd->bhqk", q, k) * (Dh ** -0.5)
        p = _softmax(scores, axis=-1)
        o = torch.einsum("bhqk,bhkd->bhqd", p, v)
        return (lambda x, *d: x.permute(*d))(o, 0, 2, 1, 3).reshape(B, Sq, Hq * Dh) @ wo

    def _B_ref(x, w_gate, w_up, w_down):
        g = _silu(x @ w_gate)
        u = x @ w_up
        return (g * u) @ w_down

    x = A_x
    g = _sigmoid((x * gate_w).sum(-1, keepdims=True))
    a = _A_ref(x, A_k_cache, A_v_cache, A_wq, A_wo)
    b = _B_ref(x, B_w_gate, B_w_up, B_w_down)
    return g * a + (1 - g) * b
```

```cpp
#include <hip/hip_runtime.h>
#include <hip/hip_bf16.h>
#include <stdint.h>

constexpr int B = 8, D = 576, HQ = 32, HKV = 16, SK = 4096, DH = 128, W = 1024, F = 4608;
typedef unsigned short u16;
__device__ __forceinline__ float bf2f(u16 v) { return __builtin_bit_cast(float, (unsigned)v << 16); }
__device__ __forceinline__ u16 f2bf(float f) { __hip_bfloat16 h = __float2bfloat16(f); return __builtin_bit_cast(u16, h); }
__device__ __forceinline__ float rbf(float f) { return bf2f(f2bf(f)); }

__global__ void k_proj(const u16* x, const u16* wq, const u16* wg, const u16* wu, float* q, float* h) {
  __shared__ float xs[B * D];
  for (int i = threadIdx.x; i < B * D; i += blockDim.x) xs[i] = bf2f(x[i]);
  __syncthreads();
  int c = blockIdx.x * blockDim.x + threadIdx.x;
  if (c < HQ * DH) {
    float acc[B] = {};
    for (int k = 0; k < D; ++k) { float w = bf2f(wq[(size_t)k * 4096 + c]);
      for (int b = 0; b < B; ++b) acc[b] += xs[b * D + k] * w; }
    for (int b = 0; b < B; ++b) q[b * 4096 + c] = rbf(acc[b]);
  } else if (c < HQ * DH + F) {
    int f = c - HQ * DH;
    float g[B] = {}, u[B] = {};
    for (int k = 0; k < D; ++k) { float a = bf2f(wg[(size_t)k * F + f]), bb = bf2f(wu[(size_t)k * F + f]);
      for (int b = 0; b < B; ++b) { g[b] += xs[b * D + k] * a; u[b] += xs[b * D + k] * bb; } }
    for (int b = 0; b < B; ++b) { float gg = g[b]; h[b * F + f] = gg / (1.f + __expf(-gg)) * u[b]; }
  }
}

__global__ void k_attn(const u16* kc, const u16* vc, const float* q, float* o) {
  __shared__ float qs[DH], s[W], red[256];
  int b = blockIdx.x / HQ, h = blockIdx.x % HQ, kvh = h / 2, t = threadIdx.x;
  if (t < DH) qs[t] = q[b * 4096 + h * DH + t];
  __syncthreads();
  const u16* kb = kc + ((size_t)(b * HKV + kvh) * SK + (SK - W)) * DH;
  const u16* vb = vc + ((size_t)(b * HKV + kvh) * SK + (SK - W)) * DH;
  float m = -INFINITY;
  for (int j = t; j < W; j += 256) { float a = 0; for (int d = 0; d < DH; ++d) a += qs[d] * bf2f(kb[j * DH + d]);
    a *= 0.08838834764831845f; s[j] = a; m = fmaxf(m, a); }
  red[t] = m; __syncthreads();
  for (int st = 128; st > 0; st >>= 1) { if (t < st) red[t] = fmaxf(red[t], red[t + st]); __syncthreads(); }
  m = red[0]; __syncthreads();
  float l = 0;
  for (int j = t; j < W; j += 256) { float p = __expf(s[j] - m); s[j] = p; l += p; }
  red[t] = l; __syncthreads();
  for (int st = 128; st > 0; st >>= 1) { if (t < st) red[t] += red[t + st]; __syncthreads(); }
  l = red[0]; __syncthreads();
  int d = t & 127, half = t >> 7;
  float a = 0;
  for (int j = half * 512; j < half * 512 + 512; ++j) a += s[j] * bf2f(vb[j * DH + d]);
  red[t] = a; __syncthreads();
  if (t < 128) o[b * 4096 + h * DH + t] = (red[t] + red[t + 128]) / l;
}

__global__ void k_out(const u16* x, const u16* gw, const u16* wo, const u16* wd, const float* o, const float* h, u16* out) {
  __shared__ float red[2][256], gs[64];
  int b = blockIdx.x / 9, n = (blockIdx.x % 9) * 64 + (threadIdx.x & 63), kq = threadIdx.x >> 6, t = threadIdx.x;
  float a = 0, c = 0;
  for (int k = kq; k < 4096; k += 4) a += o[b * 4096 + k] * bf2f(wo[(size_t)k * D + n]);
  for (int k = kq; k < F; k += 4) c += h[b * F + k] * bf2f(wd[(size_t)k * D + n]);
  red[0][t] = a; red[1][t] = c;
  if (t < 64) { float g = 0; for (int k = t; k < D; k += 64) g += bf2f(x[b * D + k]) * bf2f(gw[k]); gs[t] = g; }
  __syncthreads();
  if (t < 64) {
    float g = 0; for (int i = 0; i < 64; ++i) g += gs[i];
    g = 1.f / (1.f + __expf(-g));
    a = red[0][t] + red[0][t + 64] + red[0][t + 128] + red[0][t + 192];
    c = red[1][t] + red[1][t + 64] + red[1][t + 128] + red[1][t + 192];
    out[b * D + n] = f2bf(g * a + (1.f - g) * c);
  }
}

extern "C" void kernel_launch(void* const* d_in, const int* in_sizes, int n_in, void* d_out, int out_size, void* d_ws, size_t ws_size, hipStream_t stream) {
  const u16* x = (const u16*)d_in[0]; const u16* kc = (const u16*)d_in[1]; const u16* vc = (const u16*)d_in[2];
  const u16* wq = (const u16*)d_in[3]; const u16* wo = (const u16*)d_in[4];
  const u16* wg = (const u16*)d_in[6]; const u16* wu = (const u16*)d_in[7]; const u16* wd = (const u16*)d_in[8]; const u16* gw = (const u16*)d_in[9];
  float* q = (float*)d_ws; float* h = q + B * 4096; float* o = h + B * F;
  k_proj<<<(4096 + F) / 64, 64, 0, stream>>>(x, wq, wg, wu, q, h);
  k_attn<<<B * HQ, 256, 0, stream>>>(kc, vc, q, o);
  k_out<<<B * 9, 256, 0, stream>>>(x, gw, wo, wd, o, h, (u16*)d_out);
}
```

```cpp
#include <hip/hip_runtime.h>
#include <hip/hip_bf16.h>
#include <stdint.h>

#define STAMP 0
#ifndef NV0
#define NV0 16
#endif

#define CODE_BYTES 15360
namespace {
constexpr int NB = 8, DM = 576, HKV = 16, SK = 4096, DH = 128, WIN = 1024, FF = 4608;
typedef unsigned v4u __attribute__((ext_vector_type(4)));
typedef unsigned v2u __attribute__((ext_vector_type(2)));
typedef float f32x4 __attribute__((ext_vector_type(4)));
typedef float f32x2 __attribute__((ext_vector_type(2)));
typedef short bf16x8 __attribute__((ext_vector_type(8)));
typedef __bf16 bf16x2 __attribute__((ext_vector_type(2)));
#define LAS __attribute__((address_space(3)))
#define GAS __attribute__((address_space(1)))
typedef __amdgpu_buffer_rsrc_t rsrc_t;
constexpr int AUX_NT = 2, AUX_SC1 = 16, RS3 = 0x00020000;

constexpr int WS_E = 0;
constexpr int WS_LG = 4096;
constexpr int WS_AG = 16384;
constexpr int WS_OG = WS_AG + 208 * 2048;
constexpr int WS_PG = WS_OG + 256 * 1024;
constexpr int WS_END = WS_PG + 72 * 28 * 512;
constexpr int WS_TS = 8 << 20;

constexpr int L_TAG = 0, L_CNTA = 4, L_CNTML = 8, L_CNTO = 12, L_CNTC = 16, L_FQ = 20, L_FH = 24, L_FV = 28, L_FD = 32;
constexpr int L_ML = 64;
constexpr int L_QF = 256;
constexpr int L_HF = L_QF + 1024;
constexpr int L_OR = L_HF + 6144;
constexpr int SLOT = 576 * 4;
constexpr int L_RA = L_OR + 4096;
constexpr int L_RC = L_RA + 6 * SLOT;
constexpr int L_END = L_RC + 12 * SLOT;

struct Params {
  const void* x; const void* kc; const void* vc; const void* wq; const void* wo; const void* wg; const void* wu; const void* wd; const void* gw;
  void* out; void* ws;
};

__device__ __forceinline__ rsrc_t mk(const void* p, unsigned bytes) { return __builtin_amdgcn_make_buffer_rsrc((void*)p, 0, (int)bytes, RS3); }
template <int AUX> __device__ __forceinline__ v4u ld16(rsrc_t r, int vo, int so) { return __builtin_amdgcn_raw_buffer_load_b128(r, vo, so, AUX); }
__device__ __forceinline__ f32x4 mfma(v4u a, v4u b, f32x4 c) {
  return __builtin_amdgcn_mfma_f32_16x16x32_bf16(__builtin_bit_cast(bf16x8, a), __builtin_bit_cast(bf16x8, b), c, 0, 0, 0);
}
__device__ __forceinline__ unsigned cvtpk(float lo, float hi) {
  f32x2 v = {lo, hi};
  return __builtin_bit_cast(unsigned, __builtin_convertvector(v, bf16x2));
}
__device__ __forceinline__ float dot2(unsigned a, unsigned b, float c) {
  return __builtin_amdgcn_fdot2_f32_bf16(__builtin_bit_cast(bf16x2, a), __builtin_bit_cast(bf16x2, b), c, false);
}
__device__ __forceinline__ float bflo(unsigned u) { return __builtin_bit_cast(float, u << 16); }
__device__ __forceinline__ float bfhi(unsigned u) { return __builtin_bit_cast(float, u & 0xffff0000u); }
template <int CTRL> __device__ __forceinline__ float dpp(float x) {
  return __builtin_bit_cast(float, __builtin_amdgcn_mov_dpp(__builtin_bit_cast(int, x), CTRL, 0xf, 0xf, true));
}
__device__ __forceinline__ float xrow_max(float x) {
  auto s = __builtin_amdgcn_permlane16_swap(__float_as_uint(x), __float_as_uint(x), false, false);
  x = fmaxf(__uint_as_float(s[0]), __uint_as_float(s[1]));
  auto t = __builtin_amdgcn_permlane32_swap(__float_as_uint(x), __float_as_uint(x), false, false);
  return fmaxf(__uint_as_float(t[0]), __uint_as_float(t[1]));
}
__device__ __forceinline__ float xhalf_sum(float x) {
  auto t = __builtin_amdgcn_permlane32_swap(__float_as_uint(x), __float_as_uint(x), false, false);
  return __uint_as_float(t[0]) + __uint_as_float(t[1]);
}
__device__ __forceinline__ float xrow_sum(float x) {
  auto s = __builtin_amdgcn_permlane16_swap(__float_as_uint(x), __float_as_uint(x), false, false);
  x = __uint_as_float(s[0]) + __uint_as_float(s[1]);
  return xhalf_sum(x);
}
#define SBAR() __builtin_amdgcn_sched_barrier(0)
#define MEMFENCE() asm volatile("" ::: "memory")
#define LGKM0() asm volatile("s_waitcnt lgkmcnt(0)" ::: "memory")

constexpr unsigned SPIN_G = 1u << 16;
constexpr unsigned SPIN_L = 1u << 22;

__device__ __forceinline__ unsigned lds_ld(LAS unsigned char* L, int off) { return __hip_atomic_load((LAS unsigned*)(L + off), __ATOMIC_RELAXED, __HIP_MEMORY_SCOPE_WORKGROUP); }
__device__ __forceinline__ void lds_wait_ge(LAS unsigned char* L, int off, unsigned want) {
#pragma nounroll
  for (unsigned n = 0; n < SPIN_L; ++n) {
    if ((unsigned)__builtin_amdgcn_readfirstlane(lds_ld(L, off)) >= want) break;
    __builtin_amdgcn_s_sleep(1);
  }
  MEMFENCE();
}
__device__ __forceinline__ unsigned lds_arrive(LAS unsigned char* L, int off, int lane) {
  LGKM0();
  unsigned old = 0;
  if (lane == 0) old = __hip_atomic_fetch_add((LAS unsigned*)(L + off), 1u, __ATOMIC_RELAXED, __HIP_MEMORY_SCOPE_WORKGROUP);
  old = __builtin_amdgcn_readfirstlane(old);
  MEMFENCE();
  return old;
}

__device__ __forceinline__ void tile_mma(const v4u (&r)[8], v4u a, f32x4 (&acc)[8]) {
#pragma unroll
  for (int j = 0; j < 8; ++j) {
    v4u bfr;
#pragma unroll
    for (int m = 0; m < 4; ++m) bfr[m] = __builtin_amdgcn_perm(r[2 * m + 1][j >> 1], r[2 * m][j >> 1], (j & 1) ? 0x07060302u : 0x05040100u);
    acc[j] = mfma(a, bfr, acc[j]);
  }
}
__device__ __forceinline__ void tile_to_lds(LAS unsigned char* L, int base, int lane, const f32x4 (&acc)[8]) {
  const int G = lane >> 4, n = lane & 15, set = n >> 3, cb = n & 7;
  if ((G >> 1) == set) {
    LAS unsigned char* p = L + base + set * SLOT + ((G & 1) * 4 * 32 + cb * 4) * 4;
#pragma unroll
    for (int r = 0; r < 4; ++r) {
      *(LAS f32x4*)(p + r * 128) = f32x4{acc[0][r], acc[1][r], acc[2][r], acc[3][r]};
      *(LAS f32x4*)(p + r * 128 + 288 * 4) = f32x4{acc[4][r], acc[5][r], acc[6][r], acc[7][r]};
    }
  }
}

#define TS(i)

__global__ void __launch_bounds__(512, 2) fused_kernel(Params p) {
  __shared__ __attribute__((aligned(16))) unsigned char smem[L_END];
  LAS unsigned char* L = (LAS unsigned char*)smem;
  unsigned long long pc0;
  asm volatile("s_getpc_b64 %0" : "=s"(pc0));
  pc0 &= ~127ull;
  const int tid = threadIdx.x, lane = tid & 63, wv = __builtin_amdgcn_readfirstlane(tid >> 6);
  const int wg = blockIdx.x, xcd = wg & 7, ii = wg >> 3, grp = ii >> 4, mem = ii & 15;
  const int kvh = 2 * xcd + grp;
  const int G = lane >> 4, n16 = lane & 15;
  const rsrc_t rws = mk(p.ws, WS_END);
  const bool isO = mem < 9;
  const int dn = (15 - mem) * 16 + ((kvh >> 1) | ((kvh & 1) << 3));
  const bool isD = !isO && dn < 108;
  const int dq = dn / 9;
  const int cc = isO ? mem : dn - 9 * dq, chunk = isO ? kvh : 16 + dq;
  const int tile_vo = ((n16 >> 3) * 32 + G * 8) * (DM * 2) + (n16 & 7) * 16;
  TS(0);

  if (wv < 3) {
    __builtin_amdgcn_s_setprio(3);
    const int a = grp * 13 + mem;
    if (mem < 13) {
      const void* wbase = a < 8 ? p.wq : a < 17 ? p.wg : p.wu;
      const int rs = a < 8 ? 8192 : 9216;
      const int colblk = xcd + 8 * (a < 8 ? a : a < 17 ? a - 8 : a - 17);
      const int strip = colblk + (a < 8 ? 0 : a < 17 ? 64 : 136);
      const rsrc_t rw = mk(wbase, DM * rs);
      const rsrc_t rx = mk(p.x, NB * DM * 2);
      const int vo = ((n16 >> 3) * 32 + G * 8) * rs + (n16 & 7) * 16 + colblk * 128;
      const int so = wv * 192 * rs;
      v4u r[3][8], xa[3];
#pragma unroll
      for (int t = 0; t < 3; ++t) {
#pragma unroll
        for (int e = 0; e < 8; ++e) r[t][e] = ld16<AUX_NT>(rw, vo, so + (t * 64 + e) * rs);
        if (t == 0) {
#pragma unroll
          for (int u = 0; u < 3; ++u) xa[u] = ld16<0>(rx, (n16 & 7) * (DM * 2) + ((n16 >> 3) * 32 + G * 8 + u * 64) * 2, wv * 192 * 2);
        }
      }
      SBAR();
      __builtin_amdgcn_s_barrier();
      __builtin_amdgcn_s_setprio(0);
      SBAR();
      TS(1);
      f32x4 acc[8];
#pragma unroll
      for (int j = 0; j < 8; ++j) acc[j] = f32x4{0.f, 0.f, 0.f, 0.f};
#pragma unroll
      for (int t = 0; t < 3; ++t) tile_mma(r[t], xa[t], acc);
      tile_to_lds(L, L_RA + wv * 2 * SLOT, lane, acc);
      TS(2);
      if (lds_arrive(L, L_CNTA, lane) == 2) {
        const unsigned tag = lds_ld(L, L_TAG);
#pragma unroll
        for (int i = 0; i < 2; ++i) {
          const int b = i * 4 + G, u = n16 >> 3, c8 = n16 & 7;
          f32x4 s = {0.f, 0.f, 0.f, 0.f};
#pragma unroll
          for (int sl = 0; sl < 6; ++sl) s += *(LAS f32x4*)(L + L_RA + sl * SLOT + (u * 288 + b * 32 + c8 * 4) * 4);
          const v4u g = {cvtpk(s[0], s[1]), tag, cvtpk(s[2], s[3]), tag};
          __builtin_amdgcn_raw_buffer_store_b128(g, rws, lane * 16 + i * 1024, WS_AG + strip * 2048, AUX_SC1);
        }
        TS(3);
      }
    } else {
      __builtin_amdgcn_s_barrier();
      __builtin_amdgcn_s_setprio(0);
    }
    if (isD && wv < 2) {
      lds_wait_ge(L, L_FV, 4);
      const rsrc_t rc = mk((const char*)p.wd + (size_t)(dq * 384 + (4 + wv) * 64) * (DM * 2) + cc * 128, 64 * DM * 2);
      v4u c[8];
#pragma unroll
      for (int e = 0; e < 8; ++e) c[e] = ld16<AUX_NT>(rc, tile_vo, e * (DM * 2));
      SBAR();
      lds_wait_ge(L, L_FH, 1);
      const v4u af = *(LAS v4u*)(L + L_HF + (4 + wv) * 1024 + lane * 16);
      f32x4 acc[8];
#pragma unroll
      for (int j = 0; j < 8; ++j) acc[j] = f32x4{0.f, 0.f, 0.f, 0.f};
      tile_mma(c, af, acc);
      tile_to_lds(L, L_RC + (4 + wv) * 2 * SLOT, lane, acc);
      lds_arrive(L, L_CNTC, lane);
    }
    return;
  }

  if (wv == 3) {
    const unsigned e0 = __builtin_amdgcn_raw_buffer_load_b32(rws, 0, WS_E, AUX_SC1);
    {
      const v4u cp = ld16<0>(mk((const void*)pc0, CODE_BYTES), lane * 16, (ii & 15) * 1024);
      asm volatile("" ::"v"(cp));
    }
    if (lane < 15) *(LAS unsigned*)(L + 4 + lane * 4) = 0u;
    const unsigned tag = __builtin_amdgcn_readfirstlane(e0) + 1u;
    if (lane == 0) *(LAS unsigned*)(L + L_TAG) = tag;
    LGKM0();
    __builtin_amdgcn_s_barrier();
    TS(1);
    const int b = mem >> 1;
    {
      const int c16 = 4 * G + (n16 & 3), h = (n16 >> 2) & 1;
      const int vo = (kvh * 4 + 2 * h + (c16 >> 3)) * 2048 + b * 256 + (c16 & 7) * 16;
      v4u g0, g1;
#pragma nounroll
      for (unsigned n = 0; n < SPIN_G; ++n) {
        MEMFENCE();
        g0 = ld16<AUX_SC1>(rws, vo, WS_AG);
        g1 = ld16<AUX_SC1>(rws, vo + 128, WS_AG);
        const bool ok = g0[1] == tag && g0[3] == tag && g1[1] == tag && g1[3] == tag;
        if (__all(ok)) break;
        __builtin_amdgcn_s_sleep(2);
      }
      *(LAS v4u*)(L + L_QF + lane * 16) = v4u{g0[0], g0[2], g1[0], g1[2]};
      lds_arrive(L, L_FQ, lane);
      TS(2);
    }
    if (isD) {
      const int vo = (n16 & 7) * 256 + ((n16 >> 3) * 4 + G) * 16;
      const int so = WS_AG + (64 + dq * 6) * 2048;
      v4u gg[6][2], uu[6][2];
#pragma nounroll
      for (unsigned n = 0; n < SPIN_G; ++n) {
        MEMFENCE();
        bool ok = true;
#pragma unroll
        for (int t = 0; t < 6; ++t)
#pragma unroll
          for (int u = 0; u < 2; ++u) {
            gg[t][u] = ld16<AUX_SC1>(rws, vo + u * 128, so + t * 2048);
            uu[t][u] = ld16<AUX_SC1>(rws, vo + u * 128, so + (72 + t) * 2048);
          }
#pragma unroll
        for (int t = 0; t < 6; ++t)
#pragma unroll
          for (int u = 0; u < 2; ++u) ok = ok && gg[t][u][1] == tag && gg[t][u][3] == tag && uu[t][u][1] == tag && uu[t][u][3] == tag;
        if (__all(ok)) break;
        __builtin_amdgcn_s_sleep(8);
      }
#pragma unroll
      for (int t = 0; t < 6; ++t) {
        v4u hf;
#pragma unroll
        for (int d = 0; d < 4; ++d) {
          const unsigned gp = gg[t][d >> 1][(d & 1) * 2], up = uu[t][d >> 1][(d & 1) * 2];
          const float g0 = bflo(gp), g1 = bfhi(gp);
          const float h0 = g0 * __builtin_amdgcn_rcpf(1.f + __builtin_amdgcn_exp2f(-1.4426950408889634f * g0)) * bflo(up);
          const float h1 = g1 * __builtin_amdgcn_rcpf(1.f + __builtin_amdgcn_exp2f(-1.4426950408889634f * g1)) * bfhi(up);
          hf[d] = cvtpk(h0, h1);
        }
        *(LAS v4u*)(L + L_HF + t * 1024 + lane * 16) = hf;
      }
      lds_arrive(L, L_FH, lane);
      TS(3);
    }
    if (ii < 9) {
      const int rb = xcd, rc = ii;
      const rsrc_t rx = mk((const char*)p.x + rb * DM * 2, DM * 2), rg = mk(p.gw, DM * 2);
      const v4u x0 = ld16<0>(rx, lane * 16, 0), x1 = ld16<0>(rx, lane * 16, 1024), w0 = ld16<0>(rg, lane * 16, 0), w1 = ld16<0>(rg, lane * 16, 1024);
      float gs = 0.f;
#pragma unroll
      for (int d = 0; d < 4; ++d) {
        gs = dot2(x0[d], w0[d], gs);
        gs = dot2(x1[d], w1[d], gs);
      }
      gs += dpp<0xB1>(gs); gs += dpp<0x4E>(gs); gs += dpp<0x141>(gs); gs += dpp<0x128>(gs);
      gs = xrow_sum(gs);
      const float gate = __builtin_amdgcn_rcpf(1.f + __builtin_amdgcn_exp2f(-1.4426950408889634f * gs));
      lds_wait_ge(L, L_FD, 1);
      TS(4);
      const int so = WS_PG + (rc * 8 + rb) * (28 * 512);
      v4u pg[14];
#pragma nounroll
      for (unsigned n = 0; n < SPIN_G; ++n) {
        MEMFENCE();
        bool ok = true;
#pragma unroll
        for (int i = 0; i < 14; ++i) pg[i] = ld16<AUX_SC1>(rws, lane * 16 + (i & 3) * 1024, so + (i >> 2) * 4096);
#pragma unroll
        for (int i = 0; i < 14; ++i) ok = ok && pg[i][1] == tag && pg[i][3] == tag;
        if (__all(ok)) break;
      }
      float a0 = 0.f, a1 = 0.f, b0 = 0.f, b1 = 0.f;
#pragma unroll
      for (int i = 0; i < 8; ++i) { a0 += __uint_as_float(pg[i][0]); a1 += __uint_as_float(pg[i][2]); }
#pragma unroll
      for (int i = 8; i < 14; ++i) { b0 += __uint_as_float(pg[i][0]); b1 += __uint_as_float(pg[i][2]); }
      const float o0 = xhalf_sum(gate * a0 + (1.f - gate) * b0), o1 = xhalf_sum(gate * a1 + (1.f - gate) * b1);
      if (lane < 32) ((GAS unsigned*)p.out)[(rb * DM + rc * 64) / 2 + lane] = cvtpk(o0, o1);
      if (wg == 0 && lane == 0) __builtin_amdgcn_raw_buffer_store_b32(tag, rws, 0, WS_E, AUX_SC1);
      TS(5);
    }
    return;
  }

  {
    const int w = wv - 4, b = mem >> 1, sp = mem & 1;
    const size_t kvoff = ((size_t)(b * HKV + kvh) * SK + (SK - WIN) + sp * 512) * (DH * 2) + w * 1024;
    const rsrc_t rk = mk((const char*)p.kc + kvoff, 131072 - w * 1024), rv = mk((const char*)p.vc + kvoff, 131072 - w * 1024);
    const int kvo = (n16 >> 2) * 256 + (G * 4 + (n16 & 3)) * 16, vvo = lane * 16;
    __builtin_amdgcn_s_barrier();
    SBAR();
    TS(1);
    v4u K[32], V[4][8];
#pragma unroll
    for (int j = 0; j < 32; ++j) K[j] = ld16<AUX_NT>(rk, kvo, j * 4096);
#pragma unroll
    for (int j = 0; j < NV0; ++j) V[j >> 3][j & 7] = ld16<AUX_NT>(rv, vvo, j * 4096);
    SBAR();
    TS(2);
    lds_wait_ge(L, L_FQ, 1);
    TS(3);
    const v4u qf = *(LAS v4u*)(L + L_QF + lane * 16);
    const unsigned tag = lds_ld(L, L_TAG);
    float s[32];
    int vi = NV0;
#define ISSUE_V(cnt) do { SBAR(); _Pragma("unroll") for (int z_ = 0; z_ < (cnt); ++z_) { if (vi < 32) { V[vi >> 3][vi & 7] = ld16<AUX_NT>(rv, vvo, vi * 4096); ++vi; } } SBAR(); } while (0)
#pragma unroll
    for (int j = 0; j < 32; ++j) {
      const f32x4 d = mfma(K[j], qf, f32x4{0.f, 0.f, 0.f, 0.f});
      s[j] = dpp<0xFF>(d[3]) + (dpp<0xAA>(d[2]) + (dpp<0x55>(d[1]) + d[0]));
      if (j == 15) ISSUE_V(2);
    }
    ISSUE_V(2);
    float m = s[0];
#pragma unroll
    for (int j = 1; j < 32; ++j) m = fmaxf(m, s[j]);
    m = xrow_max(m);
    constexpr float C2 = 0.08838834764831845f * 1.4426950408889634f;
    const float mc = m * C2;
    float l = 0.f;
    v4u pf[4];
#pragma unroll
    for (int j = 0; j < 32; j += 2) {
      const float p0 = __builtin_amdgcn_exp2f(fmaf(s[j], C2, -mc)), p1 = __builtin_amdgcn_exp2f(fmaf(s[j + 1], C2, -mc));
      l += p0 + p1;
      pf[j >> 3][(j & 7) >> 1] = cvtpk(p0, p1);
      if (j == 14) ISSUE_V(2);
    }
    l = xrow_sum(l);
    if (lane == 0 || lane == 4) *(LAS f32x2*)(L + L_ML + (w * 2 + (lane >> 2)) * 8) = f32x2{mc, l};
    lds_arrive(L, L_CNTML, lane);
    TS(4);
    f32x4 acc[8];
#pragma unroll
    for (int j = 0; j < 8; ++j) acc[j] = f32x4{0.f, 0.f, 0.f, 0.f};
    ISSUE_V(2);
    tile_mma(V[0], pf[0], acc);
    ISSUE_V(4);
    tile_mma(V[1], pf[1], acc);
    ISSUE_V(32);
    if (lane == 0) __hip_atomic_fetch_add((LAS unsigned*)(L + L_FV), 1u, __ATOMIC_RELAXED, __HIP_MEMORY_SCOPE_WORKGROUP);
    TS(5);
    lds_wait_ge(L, L_CNTML, 4);
    float fw, lse;
    {
      const int h = G & 1;
      f32x2 ml[4];
#pragma unroll
      for (int u = 0; u < 4; ++u) ml[u] = *(LAS f32x2*)(L + L_ML + (u * 2 + h) * 8);
      const float M = fmaxf(fmaxf(ml[0][0], ml[1][0]), fmaxf(ml[2][0], ml[3][0]));
      float Ls = 0.f;
#pragma unroll
      for (int u = 0; u < 4; ++u) Ls += ml[u][1] * __builtin_amdgcn_exp2f(ml[u][0] - M);
      const float mw = w == 0 ? ml[0][0] : w == 1 ? ml[1][0] : w == 2 ? ml[2][0] : ml[3][0];
      fw = __builtin_amdgcn_exp2f(mw - M) * __builtin_amdgcn_rcpf(Ls);
      lse = M + __builtin_amdgcn_logf(Ls);
    }
    if (w == 0 && (lane == 0 || lane == 16))
      __builtin_amdgcn_raw_buffer_store_b64(v2u{__float_as_uint(lse), tag}, rws, (G * 16 + b * 2 + sp) * 8, WS_LG + kvh * 256, AUX_SC1);
    const bool hasC = isO || isD;
    const char* cb = isO ? (const char*)p.wo + (size_t)(kvh * 256 + w * 64) * (DM * 2) : (const char*)p.wd + (size_t)(dq * 384 + w * 64) * (DM * 2);
    const rsrc_t rc = mk(cb + cc * 128, hasC ? 64 * DM * 2 : 0);
    v4u c[8];
    SBAR();
#pragma unroll
    for (int e = 0; e < 4; ++e) c[e] = ld16<AUX_NT>(rc, tile_vo, e * (DM * 2));
    SBAR();
    tile_mma(V[2], pf[2], acc);
    SBAR();
#pragma unroll
    for (int e = 4; e < 8; ++e) c[e] = ld16<AUX_NT>(rc, tile_vo, e * (DM * 2));
    SBAR();
    tile_mma(V[3], pf[3], acc);
    TS(6);
    if (lane < 32) {
      LAS unsigned char* q = L + L_OR + w * 1024 + lane * 32;
      *(LAS f32x4*)(q) = f32x4{acc[0][0] * fw, acc[1][0] * fw, acc[2][0] * fw, acc[3][0] * fw};
      *(LAS f32x4*)(q + 16) = f32x4{acc[4][0] * fw, acc[5][0] * fw, acc[6][0] * fw, acc[7][0] * fw};
    }
    if (lds_arrive(L, L_CNTO, lane) == 3) {
      const int qd = 16 * G + 8 * ((lane >> 2) & 1) + 2 * (lane & 3) + ((lane >> 3) & 1);
      f32x4 o = {0.f, 0.f, 0.f, 0.f};
#pragma unroll
      for (int u = 0; u < 4; ++u) o += *(LAS f32x4*)(L + L_OR + u * 1024 + qd * 16);
      const v4u g = {cvtpk(o[0], o[1]), tag, cvtpk(o[2], o[3]), tag};
      __builtin_amdgcn_raw_buffer_store_b128(g, rws, lane * 16, WS_OG + (kvh * 16 + mem) * 1024, AUX_SC1);
    }
    TS(7);
    if (!hasC) return;
    v4u af;
    if (isO) {
      const int ob = n16 & 7;
      const int vo = ob * 2048 + ((n16 >> 3) * 4 + G) * 16;
      const int so = WS_OG + kvh * 16384 + w * 256;
      v4u o0[2], o1[2], lg;
#pragma nounroll
      for (unsigned n = 0; n < SPIN_G; ++n) {
        MEMFENCE();
        lg = ld16<AUX_SC1>(rws, ob * 16, WS_LG + kvh * 256 + (w >> 1) * 128);
        o0[0] = ld16<AUX_SC1>(rws, vo, so);
        o0[1] = ld16<AUX_SC1>(rws, vo + 128, so);
        o1[0] = ld16<AUX_SC1>(rws, vo + 1024, so);
        o1[1] = ld16<AUX_SC1>(rws, vo + 1024 + 128, so);
        const bool ok = lg[1] == tag && lg[3] == tag && o0[0][1] == tag && o0[0][3] == tag && o0[1][1] == tag && o0[1][3] == tag &&
                        o1[0][1] == tag && o1[0][3] == tag && o1[1][1] == tag && o1[1][3] == tag;
        if (__all(ok)) break;
      }
      const float w0 = __builtin_amdgcn_rcpf(1.f + __builtin_amdgcn_exp2f(__uint_as_float(lg[2]) - __uint_as_float(lg[0])));
      const float w1 = __builtin_amdgcn_rcpf(1.f + __builtin_amdgcn_exp2f(__uint_as_float(lg[0]) - __uint_as_float(lg[2])));
#pragma unroll
      for (int d = 0; d < 4; ++d) {
        const unsigned x0 = o0[d >> 1][(d & 1) * 2], x1 = o1[d >> 1][(d & 1) * 2];
        af[d] = cvtpk(fmaf(w1, bflo(x1), w0 * bflo(x0)), fmaf(w1, bfhi(x1), w0 * bfhi(x0)));
      }
    } else {
      lds_wait_ge(L, L_FH, 1);
      af = *(LAS v4u*)(L + L_HF + w * 1024 + lane * 16);
    }
    TS(8);
#pragma unroll
    for (int j = 0; j < 8; ++j) acc[j] = f32x4{0.f, 0.f, 0.f, 0.f};
    tile_mma(c, af, acc);
    tile_to_lds(L, L_RC + w * 2 * SLOT, lane, acc);
    lds_arrive(L, L_CNTC, lane);
    lds_wait_ge(L, L_CNTC, isO ? 4u : 6u);
    {
      const int pb = 2 * w + (lane >> 5), c2 = lane & 31;
      LAS unsigned char* q = L + L_RC + (((c2 >> 1) & 1) * 288 + pb * 32 + (c2 >> 2) * 4 + (c2 & 1) * 2) * 4;
      f32x2 t = {0.f, 0.f};
#pragma unroll
      for (int sl = 0; sl < 8; ++sl) t += *(LAS f32x2*)(q + sl * SLOT);
      if (!isO) {
#pragma unroll
        for (int sl = 8; sl < 12; ++sl) t += *(LAS f32x2*)(q + sl * SLOT);
      }
      const v4u g = {__float_as_uint(t[0]), tag, __float_as_uint(t[1]), tag};
      __builtin_amdgcn_raw_buffer_store_b128(g, rws, pb * (28 * 512) + c2 * 16, WS_PG + cc * 8 * (28 * 512) + chunk * 512, AUX_SC1);
      if (lane == 0) __hip_atomic_fetch_add((LAS unsigned*)(L + L_FD), 1u, __ATOMIC_RELAXED, __HIP_MEMORY_SCOPE_WORKGROUP);
    }
    TS(9);
  }
}
}

extern "C" void kernel_launch(void* const* d_in, const int* in_sizes, int n_in, void* d_out, int out_size, void* d_ws, size_t ws_size,
                              hipStream_t stream) {
  Params p;
  p.x = d_in[0]; p.kc = d_in[1]; p.vc = d_in[2]; p.wq = d_in[3]; p.wo = d_in[4];
  p.wg = d_in[6]; p.wu = d_in[7]; p.wd = d_in[8]; p.gw = d_in[9];
  p.out = d_out; p.ws = d_ws;
  fused_kernel<<<256, 512, 0, stream>>>(p);
}
```

```cpp
#include <hip/hip_runtime.h>
#include <hip/hip_bf16.h>
#include <stdint.h>

#define STAMP 0
#define WW 8
#define WLO 6
#define LAG 40
#define WS 11
#define XPOS 5
#define CSTEP 2
#define TV 1220
#define PBASE 12
#define CHK 4
#define QT0 600
#define GT0 800
#define LSEPOS 2
#define CODE_BYTES 20736

namespace {
constexpr int NB = 8, DM = 576, HKV = 16, SK = 4096, DH = 128, WIN = 1024;
typedef unsigned v4u __attribute__((ext_vector_type(4)));
typedef unsigned v2u __attribute__((ext_vector_type(2)));
typedef float f32x4 __attribute__((ext_vector_type(4)));
typedef float f32x2 __attribute__((ext_vector_type(2)));
typedef short bf16x8 __attribute__((ext_vector_type(8)));
typedef __bf16 bf16x2 __attribute__((ext_vector_type(2)));
#define LAS __attribute__((address_space(3)))
#define GAS __attribute__((address_space(1)))
typedef __amdgpu_buffer_rsrc_t rsrc_t;
constexpr int AUX_NT = 2, AUX_SC1 = 16, RS3 = 0x00020000;

constexpr int WS_E = 0;
constexpr int WS_GF = 4096;
constexpr int WS_LS = WS_GF + 144 * 128;
constexpr int WS_PF = WS_LS + 256 * 128;
constexpr int WS_QG = WS_PF + 252 * 128;
constexpr int WS_GU = WS_QG + 64 * 2048;
constexpr int WS_OG = WS_GU + 144 * 1024;
constexpr int WS_PS = WS_OG + 256 * 1024;
constexpr int WS_END = WS_PS + 36 * 28 * 512;
constexpr int WS_TS = 8 << 20;

constexpr int L_TAG = 0, L_CNTA = 4, L_CNTML = 8, L_FQ = 12, L_FG = 16, L_FT = 20, L_GATE = 24;
constexpr int L_ML = 64;
constexpr int L_QF = 256;
constexpr int ORW = 8 * 36 * 4;
constexpr int L_OR = L_QF + 1024;
constexpr int SLOT = 576 * 4;
constexpr int L_RA = L_OR + 4 * ORW;
constexpr int RCJ = 1024 + 128, RCW = 4 * RCJ;
constexpr int L_RC = L_RA + 6 * SLOT;
constexpr int L_HF = L_RC + 4 * RCW;
constexpr int L_END = L_HF + 12288;

struct Params {
  const void* x; const void* kc; const void* vc; const void* wq; const void* wo; const void* wg; const void* wu; const void* wd; const void* gw;
  void* out; void* ws;
};

__device__ __forceinline__ rsrc_t mk(const void* p, unsigned bytes) { return __builtin_amdgcn_make_buffer_rsrc((void*)p, 0, (int)bytes, RS3); }
template <int AUX> __device__ __forceinline__ v4u ld16(rsrc_t r, int vo, int so) { return __builtin_amdgcn_raw_buffer_load_b128(r, vo, so, AUX); }
template <int AUX> __device__ __forceinline__ v2u ld8(rsrc_t r, int vo, int so) { return __builtin_amdgcn_raw_buffer_load_b64(r, vo, so, AUX); }
template <int AUX> __device__ __forceinline__ void st16(v4u v, rsrc_t r, int vo) { __builtin_amdgcn_raw_buffer_store_b128(v, r, vo, 0, AUX); }
template <int AUX> __device__ __forceinline__ void st8(v2u v, rsrc_t r, int vo, int so) { __builtin_amdgcn_raw_buffer_store_b64(v, r, vo, so, AUX); }
template <int AUX> __device__ __forceinline__ void st4(unsigned v, rsrc_t r, int vo, int so) { __builtin_amdgcn_raw_buffer_store_b32(v, r, vo, so, AUX); }
__device__ __forceinline__ f32x4 mfma(v4u a, v4u b, f32x4 c) {
  return __builtin_amdgcn_mfma_f32_16x16x32_bf16(__builtin_bit_cast(bf16x8, a), __builtin_bit_cast(bf16x8, b), c, 0, 0, 0);
}
__device__ __forceinline__ unsigned cvtpk(float lo, float hi) {
  f32x2 v = {lo, hi};
  return __builtin_bit_cast(unsigned, __builtin_convertvector(v, bf16x2));
}
__device__ __forceinline__ unsigned tagf(float v, unsigned tag) { return __builtin_amdgcn_perm(__float_as_uint(v), tag, 0x07060500u); }
__device__ __forceinline__ float dot2(unsigned a, unsigned b, float c) {
  return __builtin_amdgcn_fdot2_f32_bf16(__builtin_bit_cast(bf16x2, a), __builtin_bit_cast(bf16x2, b), c, false);
}
__device__ __forceinline__ float bflo(unsigned u) { return __builtin_bit_cast(float, u << 16); }
__device__ __forceinline__ float bfhi(unsigned u) { return __builtin_bit_cast(float, u & 0xffff0000u); }
template <int CTRL> __device__ __forceinline__ float dpp(float x) {
  return __builtin_bit_cast(float, __builtin_amdgcn_mov_dpp(__builtin_bit_cast(int, x), CTRL, 0xf, 0xf, true));
}
template <int CTRL> __device__ __forceinline__ unsigned dppu(unsigned x) { return (unsigned)__builtin_amdgcn_mov_dpp((int)x, CTRL, 0xf, 0xf, true); }
__device__ __forceinline__ float xrow_max(float x) {
  auto s = __builtin_amdgcn_permlane16_swap(__float_as_uint(x), __float_as_uint(x), false, false);
  x = fmaxf(__uint_as_float(s[0]), __uint_as_float(s[1]));
  auto t = __builtin_amdgcn_permlane32_swap(__float_as_uint(x), __float_as_uint(x), false, false);
  return fmaxf(__uint_as_float(t[0]), __uint_as_float(t[1]));
}
__device__ __forceinline__ float swap32_sum(float a, float b) {
  auto t = __builtin_amdgcn_permlane32_swap(__float_as_uint(a), __float_as_uint(b), false, false);
  return __uint_as_float(t[0]) + __uint_as_float(t[1]);
}
__device__ __forceinline__ float swap16_sum(float a, float b) {
  auto t = __builtin_amdgcn_permlane16_swap(__float_as_uint(a), __float_as_uint(b), false, false);
  return __uint_as_float(t[0]) + __uint_as_float(t[1]);
}
__device__ __forceinline__ float xrow_sum(float x) { x = swap16_sum(x, x); return swap32_sum(x, x); }
__device__ __forceinline__ float sigm(float x) { return __builtin_amdgcn_rcpf(1.f + __builtin_amdgcn_exp2f(-1.4426950408889634f * x)); }
#define SBAR() __builtin_amdgcn_sched_barrier(0)
#define MEMFENCE() asm volatile("" ::: "memory")
#define LGKM0() asm volatile("s_waitcnt lgkmcnt(0)" ::: "memory")
constexpr int vmc(int n) { return (n & 15) | (7 << 4) | (15 << 8) | ((n >> 4) << 14); }
#define VMWAIT(n) __builtin_amdgcn_s_waitcnt(vmc(n))
#define NOW() ((unsigned)__builtin_amdgcn_s_memrealtime())

constexpr unsigned SPIN_G = 1u << 16;
constexpr unsigned SPIN_L = 1u << 22;

__device__ __forceinline__ unsigned lds_ld(LAS unsigned char* L, int off) { return __hip_atomic_load((LAS unsigned*)(L + off), __ATOMIC_RELAXED, __HIP_MEMORY_SCOPE_WORKGROUP); }
template <int SL = 1> __device__ __forceinline__ void lds_wait_ge(LAS unsigned char* L, int off, unsigned want) {
#pragma nounroll
  for (unsigned n = 0; n < SPIN_L; ++n) {
    if ((unsigned)__builtin_amdgcn_readfirstlane(lds_ld(L, off)) >= want) break;
    __builtin_amdgcn_s_sleep(SL);
  }
  MEMFENCE();
}
__device__ __forceinline__ unsigned lds_arrive(LAS unsigned char* L, int off, int lane) {
  LGKM0();
  unsigned old = 0;
  if (lane == 0) old = __hip_atomic_fetch_add((LAS unsigned*)(L + off), 1u, __ATOMIC_RELAXED, __HIP_MEMORY_SCOPE_WORKGROUP);
  old = __builtin_amdgcn_readfirstlane(old);
  MEMFENCE();
  return old;
}
__device__ __forceinline__ unsigned plo(unsigned hi_row, unsigned lo_row) { return __builtin_amdgcn_perm(hi_row, lo_row, 0x05040100u); }
__device__ __forceinline__ unsigned phi(unsigned hi_row, unsigned lo_row) { return __builtin_amdgcn_perm(hi_row, lo_row, 0x07060302u); }
template <int J0, int J1> __device__ __forceinline__ void tile_mma(const v4u (&r)[8], v4u a, f32x4 (&acc)[8]) {
#pragma unroll
  for (int j = J0; j < J1; ++j) {
    v4u bfr;
#pragma unroll
    for (int m = 0; m < 4; ++m) bfr[m] = (j & 1) ? phi(r[2 * m + 1][j >> 1], r[2 * m][j >> 1]) : plo(r[2 * m + 1][j >> 1], r[2 * m][j >> 1]);
    acc[j] = mfma(a, bfr, acc[j]);
  }
}

#define TS(i)
#define TSDECL
#define TSOUT()

__device__ __forceinline__ unsigned hpair(unsigned gp, unsigned up) {
  const float g0 = bflo(gp), g1 = bfhi(gp);
  return cvtpk(g0 * sigm(g0) * bflo(up), g1 * sigm(g1) * bfhi(up));
}

__global__ void __launch_bounds__(512, 2) fused_kernel(Params p) {
  __shared__ __attribute__((aligned(16))) unsigned char smem[L_END + (STAMP ? 512 : 0)];
  LAS unsigned char* L = (LAS unsigned char*)smem;
  unsigned long long pc0;
  asm volatile("s_getpc_b64 %0" : "=s"(pc0));
  const unsigned t0 = (unsigned)__builtin_amdgcn_s_memrealtime();
  const int tid = threadIdx.x, lane = tid & 63, wv = __builtin_amdgcn_readfirstlane(tid >> 6);
  const int wg = blockIdx.x, xcd = wg & 7, ii = wg >> 3, grp = ii >> 4, mem = ii & 15;
  const int G = lane >> 4, n16 = lane & 15;
  TSDECL;
  TS(0);

  if (__builtin_expect(wv < 3, 1)) {
    if (mem < 3) { __builtin_amdgcn_s_barrier(); TSOUT(); return; }
    const int sa = grp * 13 + 15 - mem;
    const bool isQ = sa < 8;
    const int colblk = xcd + 8 * (isQ ? sa : sa < 17 ? sa - 8 : sa - 17);
    const int rs = isQ ? 8192 : 9216;
    const rsrc_t rw = mk(isQ ? p.wq : sa < 17 ? p.wg : p.wu, DM * rs);
    const rsrc_t rx = mk(p.x, NB * DM * 2);
    const int vo = ((n16 >> 3) * 32 + G * 8) * rs + (n16 & 7) * 16 + colblk * 128;
    int so = wv * 192 * rs;
    const int sstep = mem < 9 ? TV * 9 / (4 * 89) : TV * 9 / (4 * 81);
    unsigned snext = t0 + 2 * sstep;
    v4u r[3][8], xa[3];
#pragma unroll
    for (int i = 0; i < 24; ++i) {
      if (TV && i >= 6 && i % 3 == 0) { while ((int)(snext - NOW()) > 0) {} snext += sstep; SBAR(); }
      if (i >= WS) { if (i - WS < XPOS + 1) VMWAIT(WS + 2); else VMWAIT(WS - 1); }
      r[i >> 3][i & 7] = ld16<AUX_NT>(rw, vo, so);
      so += (i & 7) == 7 ? 57 * rs : rs;
      if (i == XPOS) {
#pragma unroll
        for (int u = 0; u < 3; ++u) xa[u] = ld16<0>(rx, (n16 & 7) * (DM * 2) + ((n16 >> 3) * 32 + G * 8 + u * 64) * 2, wv * 192 * 2);
      }
      SBAR();
    }
    __builtin_amdgcn_s_barrier();
    SBAR();
    TS(1);
    f32x4 acc[8];
#pragma unroll
    for (int j = 0; j < 8; ++j) acc[j] = f32x4{0.f, 0.f, 0.f, 0.f};
#pragma unroll
    for (int t = 0; t < 3; ++t) { tile_mma<0, 8>(r[t], xa[t], acc); SBAR(); }
    TS(4);
    if ((G >> 1) == (n16 >> 3)) {
      LAS unsigned char* q = L + L_RA + (wv * 2 + (n16 >> 3)) * SLOT + ((G & 1) * 4 * 32 + (n16 & 7) * 4) * 4;
#pragma unroll
      for (int rr = 0; rr < 4; ++rr) {
        *(LAS f32x4*)(q + rr * 128) = f32x4{acc[0][rr], acc[1][rr], acc[2][rr], acc[3][rr]};
        *(LAS f32x4*)(q + rr * 128 + 288 * 4) = f32x4{acc[4][rr], acc[5][rr], acc[6][rr], acc[7][rr]};
      }
    }
    TS(2);
    if (lds_arrive(L, L_CNTA, lane) == 2) {
      const rsrc_t rws = mk(p.ws, WS_END);
      lds_wait_ge(L, L_FT, 1);
      const unsigned tag = lds_ld(L, L_TAG);
      const int i8 = lane & 7, rb = isQ ? lane >> 3 : i8, q0 = isQ ? 2 * i8 : lane >> 3, q1 = isQ ? q0 + 1 : q0 + 8;
      const LAS unsigned char* p0 = L + L_RA + ((q0 & 1) * 288 + rb * 32 + (q0 >> 1) * 4) * 4;
      const LAS unsigned char* p1 = L + L_RA + ((q1 & 1) * 288 + rb * 32 + (q1 >> 1) * 4) * 4;
      f32x4 s0 = {0.f, 0.f, 0.f, 0.f}, s1 = {0.f, 0.f, 0.f, 0.f};
#pragma unroll
      for (int sl = 0; sl < 6; ++sl) { s0 += *(const LAS f32x4*)(p0 + sl * SLOT); s1 += *(const LAS f32x4*)(p1 + sl * SLOT); }
      const unsigned c0 = cvtpk(s0[0], s0[1]), c1 = cvtpk(s0[2], s0[3]), c2 = cvtpk(s1[0], s1[1]), c3 = cvtpk(s1[2], s1[3]);
      if (isQ) {
        const int o = WS_QG + colblk * 2048 + (lane >> 3) * 256 + i8 * 16;
        st16<AUX_SC1>(v4u{c0, tag, c1, tag}, rws, o);
        st16<AUX_SC1>(v4u{c2, tag, c3, tag}, rws, o + 128);
      } else {
        const int gs = colblk + (sa < 17 ? 0 : 72);
        st16<AUX_SC1>(v4u{c0, c1, c2, c3}, rws, WS_GU + gs * 1024 + lane * 16);
        asm volatile("s_waitcnt vmcnt(0)" ::: "memory");
        if (lane == 0) st4<AUX_SC1>(tag, rws, 0, WS_GF + gs * 128);
      }
      TS(3);
    }
    TSOUT();
    return;
  }

  const int kvh = 2 * xcd + grp;
  const rsrc_t rws = mk(p.ws, WS_END);
  const bool isO = mem < 9;
  const int du = (mem - 9) * 16 + ((kvh >> 1) | ((kvh & 1) << 3));
  const int dq = (du * 57) >> 9;
  const bool isD = !isO && du < 108;
  const int cc = isO ? mem : du - 9 * dq, chunk = isO ? kvh : 16 + dq;

  if (wv == 3) {
    const unsigned e0 = __builtin_amdgcn_raw_buffer_load_b32(rws, 0, WS_E, AUX_SC1);
    const v4u cp = ld16<0>(mk((const void*)(pc0 & ~127ull), CODE_BYTES), lane * 16, ii * 1024);
    if (lane < 5) *(LAS unsigned*)(L + 4 + lane * 4) = 0u;
    LGKM0();
    SBAR();
    __builtin_amdgcn_s_barrier();
    SBAR();
    const unsigned tag = __builtin_amdgcn_readfirstlane(e0) + 1u;
    if (lane == 0) *(LAS unsigned*)(L + L_TAG) = tag;
    lds_arrive(L, L_FT, lane);
    asm volatile("" ::"v"(cp));
    TS(1);
    if (ii < 9) {
      const int rb = (xcd >> 1) * 2 + (lane >> 5), l32 = lane & 31;
      const rsrc_t rx = mk(p.x, NB * DM * 2), rg = mk(p.gw, DM * 2);
      float gs = 0.f;
      v4u xv[3], gv[3];
#pragma unroll
      for (int u = 0; u < 3; ++u) {
        xv[u] = ld16<0>(rx, (u == 2 && l32 >= 8 ? NB : rb) * (DM * 2) + l32 * 16 + u * 512, 0);
        gv[u] = ld16<0>(rg, l32 * 16 + u * 512, 0);
      }
#pragma unroll
      for (int u = 0; u < 3; ++u)
#pragma unroll
        for (int d = 0; d < 4; ++d) gs = dot2(xv[u][d], gv[u][d], gs);
      gs += dpp<0xB1>(gs); gs += dpp<0x4E>(gs); gs += dpp<0x141>(gs); gs += dpp<0x128>(gs);
      gs = swap16_sum(gs, gs);
      if (l32 == 0) *(LAS float*)(L + L_GATE + (lane >> 5) * 4) = sigm(gs);
    }
    if (isO) {
      const unsigned z = __builtin_amdgcn_raw_buffer_load_b32(rws, lane < 16 || ii >= 9 || lane >= 44 ? WS_LS + (kvh * 16 + n16) * 128 : WS_PF + (ii * 28 + lane - 16) * 128, 0, AUX_SC1);
      asm volatile("" ::"v"(z));
    }
    while ((int)(NOW() - t0) < QT0) __builtin_amdgcn_s_sleep(4);
    {
      const int c16 = 4 * G + (n16 & 3), h = (n16 >> 2) & 1;
      const int vo = (kvh * 4 + 2 * h + (c16 >> 3)) * 2048 + (mem >> 1) * 256 + (c16 & 7) * 16;
      v4u g0, g1;
#pragma nounroll
      for (unsigned n = 0; n < SPIN_G; ++n) {
        MEMFENCE();
        g0 = ld16<AUX_SC1>(rws, vo, WS_QG);
        g1 = ld16<AUX_SC1>(rws, vo + 128, WS_QG);
        const bool ok = g0[1] == tag && g0[3] == tag && g1[1] == tag && g1[3] == tag;
        if (__all(ok)) break;
        __builtin_amdgcn_s_sleep(2);
      }
      *(LAS v4u*)(L + L_QF + lane * 16) = v4u{g0[0], g0[2], g1[0], g1[2]};
      lds_arrive(L, L_FQ, lane);
      TS(2);
    }
    if (isD) {
      while ((int)((unsigned)__builtin_amdgcn_s_memrealtime() - t0) < GT0) __builtin_amdgcn_s_sleep(4);
      const int t = dq * 6 + min(n16, 5) + (G & 1) * 72;
#pragma nounroll
      for (unsigned n = 0; n < SPIN_G; ++n) {
        MEMFENCE();
        const unsigned f = __builtin_amdgcn_raw_buffer_load_b32(rws, t * 128, WS_GF, AUX_SC1);
        if (__all(f == tag)) break;
        __builtin_amdgcn_s_sleep(8);
      }
      MEMFENCE();
      TS(3);
      const bool up = n16 >= 8;
      const int gvo = WS_GU + dq * 6144 + lane * 16;
      v4u gg[6], uu[6];
#pragma unroll
      for (int u = 0; u < 6; ++u) { gg[u] = ld16<AUX_SC1>(rws, gvo + (u & 3) * 1024, (u >> 2) * 4096); uu[u] = ld16<AUX_SC1>(rws, gvo + (u & 3) * 1024, 72 * 1024 + (u >> 2) * 4096); }
#pragma unroll
      for (int u = 0; u < 12; ++u) {
        const unsigned h0 = hpair(gg[u >> 1][(u & 1) * 2], uu[u >> 1][(u & 1) * 2]), h1 = hpair(gg[u >> 1][(u & 1) * 2 + 1], uu[u >> 1][(u & 1) * 2 + 1]);
        *(LAS v4u*)(L + L_HF + u * 1024 + lane * 16) = v4u{up ? 0u : h0, up ? 0u : h1, up ? h0 : 0u, up ? h1 : 0u};
      }
      LGKM0();
      TS(4);
    }
    TSOUT();
    return;
  }

  const int w = wv - 4, b = mem >> 1, sp = mem & 1;
  const size_t kvoff = ((size_t)(b * HKV + kvh) * SK + (SK - WIN) + sp * 512) * (DH * 2) + w * 1024;
  const rsrc_t rk = mk((const char*)p.kc + kvoff, 131072 - w * 1024), rv = mk((const char*)p.vc + kvoff, 131072 - w * 1024);
  const int kvo = (n16 >> 2) * 256 + (G * 4 + (n16 & 3)) * 16, vvo = lane * 16;
  const char* cb = isO ? (const char*)p.wo + (size_t)(kvh * 256 + w * 64) * (DM * 2) : (const char*)p.wd + (size_t)(dq * 384 + w * 96) * (DM * 2);
  const rsrc_t rc = mk(cb + cc * 128, isO ? 64 * DM * 2 - cc * 128 : isD ? 96 * DM * 2 - cc * 128 : 0);
  const int cvo = G * (isO ? 16 : 8) * (DM * 2) + n16 * 8;
  const int pstep = mem < 3 ? TV * 16 / (PBASE + 71) : isO ? TV * 16 / 89 : TV * 16 / 81;
  int pnext = (mem < 3 ? PBASE : 18) * pstep;
  bool calm = false;
#define PACE(i) do { if (TV && ((i) % CHK) == 0) { const unsigned tt_ = t0 + (pnext >> 4); pnext += CHK * pstep; \
    int d_; do d_ = (int)(tt_ - NOW()); while (d_ > 0); calm = d_ > -LAG; SBAR(); } } while (0)
  __builtin_amdgcn_s_barrier();
  SBAR();
  TS(1);
  v4u K[32], V[4][8];
#define THROTTLE(xh, xl) do { VMWAIT(WW - 1 + (xh)); if (WLO < WW && calm) VMWAIT(WLO - 1 + (xl)); } while (0)
#define ISSUE_K(j) do { PACE(j); if ((j) >= WLO) THROTTLE(0, 0); K[j] = ld16<AUX_NT>(rk, kvo, (j) * 4096); SBAR(); } while (0)
#define ISSUE_VN(j, xh, xl) do { SBAR(); PACE(j); THROTTLE(xh, xl); V[(j) >> 3][(j) & 7] = ld16<AUX_NT>(rv, vvo, (j) * 4096); SBAR(); } while (0)
#define ISSUE_V(j) ISSUE_VN(j, 0, 0)
#pragma unroll
  for (int j = 0; j < 32; ++j) ISSUE_K(j);
#pragma unroll
  for (int j = 0; j < 4; ++j) ISSUE_V(j);
  TS(2);
  lds_wait_ge(L, L_FQ, 1);
  TS(3);
  const v4u qf = *(LAS v4u*)(L + L_QF + lane * 16);
  const unsigned tag = lds_ld(L, L_TAG);
  float s[32];
#pragma unroll
  for (int j = 0; j < 32; ++j) {
    if ((j & 3) == 0) ISSUE_V(4 + (j >> 2));
    const f32x4 d = mfma(K[j], qf, f32x4{0.f, 0.f, 0.f, 0.f});
    s[j] = dpp<0xFF>(d[3]) + (dpp<0xAA>(d[2]) + (dpp<0x55>(d[1]) + d[0]));
  }
  ISSUE_V(12);
  float m = s[0];
#pragma unroll
  for (int j = 1; j < 32; ++j) m = fmaxf(m, s[j]);
  m = xrow_max(m);
  constexpr float C2 = 0.08838834764831845f * 1.4426950408889634f;
  const float mc = m * C2;
  float l = 0.f;
  v4u pf[4];
#pragma unroll
  for (int j = 0; j < 32; j += 2) {
    if ((j & 7) == 0) ISSUE_V(13 + (j >> 3));
    const float p0 = __builtin_amdgcn_exp2f(fmaf(s[j], C2, -mc)), p1 = __builtin_amdgcn_exp2f(fmaf(s[j + 1], C2, -mc));
    l += p0 + p1;
    pf[j >> 3][(j & 7) >> 1] = cvtpk(p0, p1);
  }
  ISSUE_V(17);
  l = xrow_sum(l);
  if (lane == 0 || lane == 4) *(LAS f32x2*)(L + L_ML + (w * 2 + (lane >> 2)) * 8) = f32x2{mc, l};
  LGKM0();
  if (lane == 0) __hip_atomic_fetch_add((LAS unsigned*)(L + L_CNTML), 1u, __ATOMIC_RELAXED, __HIP_MEMORY_SCOPE_WORKGROUP);
  TS(4);
  f32x4 acc[8];
#pragma unroll
  for (int j = 0; j < 8; ++j) acc[j] = f32x4{0.f, 0.f, 0.f, 0.f};
  ISSUE_V(18); tile_mma<0, 4>(V[0], pf[0], acc);
  ISSUE_V(19); tile_mma<4, 8>(V[0], pf[0], acc);

  float fw[4], lse;
#define MERGE_FACTORS() do { lds_wait_ge(L, L_CNTML, 4); f32x2 ml[4]; \
    _Pragma("unroll") for (int u = 0; u < 4; ++u) ml[u] = *(LAS f32x2*)(L + L_ML + (u * 2 + ((w == 2 ? lane : w) & 1)) * 8); \
    const float M = fmaxf(fmaxf(ml[0][0], ml[1][0]), fmaxf(ml[2][0], ml[3][0])); float Ls = 0.f; \
    _Pragma("unroll") for (int u = 0; u < 4; ++u) { fw[u] = __builtin_amdgcn_exp2f(ml[u][0] - M); Ls = fmaf(ml[u][1], fw[u], Ls); } \
    const float inv = __builtin_amdgcn_rcpf(Ls); \
    _Pragma("unroll") for (int u = 0; u < 4; ++u) fw[u] *= inv; \
    lse = M + __builtin_amdgcn_logf(Ls); } while (0)
#define STORE_LSE() do { SBAR(); st8<AUX_SC1>(v2u{__float_as_uint(lse), tag}, rws, w == 2 && lane < 2 ? lane * 8 : WS_END, WS_LS + (kvh * 16 + mem) * 128); SBAR(); } while (0)
#define ISSUE_C(e, KSR) c[e] = ld8<AUX_NT>(rc, cvo + ((e) & 3) * (DM * 2), (((e) >> 3) * (KSR) + ((e) & 4)) * (DM * 2))
#define CFRAG(dst, ks, j) _Pragma("unroll") for (int mm = 0; mm < 4; ++mm) \
    dst[mm] = ((j) & 1) ? phi(c[(ks) * 8 + 2 * mm + 1][(j) >> 1], c[(ks) * 8 + 2 * mm][(j) >> 1]) : plo(c[(ks) * 8 + 2 * mm + 1][(j) >> 1], c[(ks) * 8 + 2 * mm][(j) >> 1])
#define PERM_V3(mm) _Pragma("unroll") for (int j = 0; j < 8; ++j) bl[j][mm] = (j & 1) ? phi(V[3][2 * (mm) + 1][j >> 1], V[3][2 * (mm)][j >> 1]) : plo(V[3][2 * (mm) + 1][j >> 1], V[3][2 * (mm)][j >> 1])
#define FINISH_PV() do { \
    _Pragma("unroll") for (int j = 0; j < 8; ++j) { \
      bl[j][3] = (j & 1) ? phi(V[3][7][j >> 1], V[3][6][j >> 1]) : plo(V[3][7][j >> 1], V[3][6][j >> 1]); \
      acc[j] = mfma(pf[3], bl[j], acc[j]); } \
    TS(6); \
    if (lane < 32) { LAS float* q = (LAS float*)(L + L_OR + w * ORW) + lane; \
      _Pragma("unroll") for (int j = 0; j < 8; ++j) q[j * 36] = acc[j][0]; } \
    LGKM0(); __builtin_amdgcn_s_barrier(); MEMFENCE(); \
    if (w < 2) { const LAS float* q = (const LAS float*)(L + L_OR) + (lane & 3) * 72 + w * 16 + (lane >> 2); \
      float o0 = 0.f, o1 = 0.f; \
      _Pragma("unroll") for (int u = 0; u < 4; ++u) { o0 = fmaf(q[u * 288], fw[u], o0); o1 = fmaf(q[u * 288 + 36], fw[u], o1); } \
      st8<AUX_SC1>(v2u{cvtpk(o0, o1), tag}, rws, lane * 8, WS_OG + (kvh * 16 + mem) * 1024 + w * 512); } \
    TS(7); } while (0)

  v4u bl[8];
  f32x4 ca[4];
  f32x2 wt[4];
  if (isO) {
    v2u c[16];
    v4u bf[8];
#define MINI(x, y) ((x) < (y) ? (x) : (y))
#define MAXI(x, y) ((x) > (y) ? (x) : (y))
#define CPAIR(sl) do { SBAR(); PACE(20 + (sl)); THROTTLE(MINI(sl, WW - 1), MINI(sl, WLO - 1)); ISSUE_C(2 * (sl), 8); ISSUE_C(2 * (sl) + 1, 8); SBAR(); } while (0)
#define ISSUE_VC(j) ISSUE_VN(j, MAXI(WW - 1 - ((j) - 20), 0), MAXI(WLO - 1 - ((j) - 20), 0))
    CPAIR(0); CPAIR(1);
    MERGE_FACTORS();
    CPAIR(2); CPAIR(3);
    CPAIR(4); tile_mma<0, 4>(V[1], pf[1], acc);
    CPAIR(5); tile_mma<4, 8>(V[1], pf[1], acc);
    CPAIR(6); CPAIR(7);
    ISSUE_VC(20); ISSUE_VC(21); ISSUE_VC(22); ISSUE_VC(23); ISSUE_VC(24); ISSUE_VC(25); ISSUE_VC(26); ISSUE_VC(27);
    ISSUE_V(28);
#pragma unroll
    for (int j = 0; j < 4; ++j) { CFRAG(bf[j], 0, j); asm volatile("" : "+v"(bf[j])); }
    ISSUE_V(29);
#pragma unroll
    for (int j = 0; j < 4; ++j) { CFRAG(bf[4 + j], 1, j); asm volatile("" : "+v"(bf[4 + j])); }
    ISSUE_V(30); tile_mma<0, 4>(V[2], pf[2], acc);
    ISSUE_V(31); tile_mma<4, 8>(V[2], pf[2], acc);
    TS(5);
    SBAR(); PERM_V3(0); SBAR(); PERM_V3(1);
    if (LSEPOS == 1) STORE_LSE();
    SBAR(); PERM_V3(2);
    if (LSEPOS == 2) STORE_LSE();
    SBAR();
    if (LSEPOS == 3) { VMWAIT(0); STORE_LSE(); }
    FINISH_PV();
    const int lvo = WS_LS + (kvh * 16 + ((n16 + 4 * w) & 15)) * 128;
    v4u lg = {0u, tag + 1u, 0u, 0u};
#pragma nounroll
    for (unsigned n = 0; n < SPIN_G; ++n) {
      MEMFENCE();
      if (lane < 16 && !(lg[1] == tag && lg[3] == tag)) lg = ld16<AUX_SC1>(rws, lvo, 0);
      if (__all(lane >= 16 || (lg[1] == tag && lg[3] == tag))) break;
    }
    TS(11);
    const int ovo = WS_OG + kvh * 16384 + ((n16 >> 2) & 1) * 8192 + (n16 >> 3) * 1024 + w * 256 + G * 64 + (n16 & 3) * 16;
    v4u og[4];
#pragma unroll
    for (int u = 0; u < 4; ++u) og[u] = ld16<AUX_SC1>(rws, ovo, u * 2048);
    SBAR();
#pragma unroll
    for (int h = 0; h < 2; ++h) {
      const float own = __uint_as_float(lg[2 * h]);
      const float wo_ = __builtin_amdgcn_rcpf(1.f + __builtin_amdgcn_exp2f(dpp<0xB1>(own) - own));
#pragma unroll
      for (int s2 = 0; s2 < 2; ++s2)
        wt[h * 2 + s2] = f32x2{__builtin_bit_cast(float, __builtin_amdgcn_readlane(__builtin_bit_cast(int, wo_), s2)),
                               __builtin_bit_cast(float, __builtin_amdgcn_readlane(__builtin_bit_cast(int, wo_), 2 + s2))};
    }
    SBAR();
#pragma nounroll
    for (unsigned n = 0; n < SPIN_G; ++n) {
      unsigned bad = 0;
#pragma unroll
      for (int u = 0; u < 4; ++u) bad |= (og[u][1] ^ tag) | (og[u][3] ^ tag);
      if (__all(bad == 0)) break;
      MEMFENCE();
      if (bad != 0) {
#pragma unroll
        for (int u = 0; u < 4; ++u) og[u] = ld16<AUX_SC1>(rws, ovo, u * 2048);
      }
    }
    TS(8);
    st4<AUX_SC1>(tag, rws, w == 3 && lane == 0 ? 0 : WS_END, WS_PF + (cc * 28 + chunk) * 128);
    v4u a0, a1;
    const bool b0 = lane & 1, b1 = lane & 2;
#pragma unroll
    for (int d = 0; d < 2; ++d) {
      const unsigned r0 = og[0][2 * d], r1 = og[1][2 * d], r2 = og[2][2 * d], r3 = og[3][2 * d];
      const unsigned s0 = dppu<0xB1>(r0), s1 = dppu<0xB1>(r1), s2 = dppu<0xB1>(r2), s3 = dppu<0xB1>(r3);
      const unsigned x0 = b0 ? s1 : r0, x1 = b0 ? r1 : s0, x2 = b0 ? s3 : r2, x3 = b0 ? r3 : s2;
      const unsigned y0 = dppu<0x4E>(x0), y1 = dppu<0x4E>(x1), y2 = dppu<0x4E>(x2), y3 = dppu<0x4E>(x3);
      a0[d] = b1 ? y2 : x0; a1[d] = b1 ? x2 : y0;
      a0[2 + d] = b1 ? y3 : x1; a1[2 + d] = b1 ? x3 : y1;
    }
#pragma unroll
    for (int j = 0; j < 4; ++j) ca[j] = mfma(a0, bf[j], f32x4{0.f, 0.f, 0.f, 0.f});
#pragma unroll
    for (int j = 0; j < 4; ++j) ca[j] = mfma(a1, bf[4 + j], ca[j]);
  } else {
    v2u c[24];
    ISSUE_V(20); ISSUE_V(21);
    MERGE_FACTORS();
    ISSUE_V(22); ISSUE_V(23);
    ISSUE_V(24); tile_mma<0, 4>(V[1], pf[1], acc);
    ISSUE_V(25); tile_mma<4, 8>(V[1], pf[1], acc);
    ISSUE_V(26); ISSUE_V(27); ISSUE_V(28); ISSUE_V(29);
    ISSUE_V(30); tile_mma<0, 4>(V[2], pf[2], acc);
    ISSUE_V(31); tile_mma<4, 8>(V[2], pf[2], acc);
    TS(5);
#define CN(left) MINI((5 - (left)) * CSTEP, 24)
#define CSTEP_AT(left) do { SBAR(); VMWAIT((left) + CN(left)); _Pragma("unroll") for (int e_ = CN(left); e_ < CN((left) - 1); ++e_) ISSUE_C(e_, 32); SBAR(); } while (0)
    CSTEP_AT(5); PERM_V3(0);
    CSTEP_AT(4); PERM_V3(1);
    CSTEP_AT(3);
    CSTEP_AT(2); PERM_V3(2);
    CSTEP_AT(1);
    STORE_LSE();
    FINISH_PV();
    if (!isD) { TSOUT(); return; }
#pragma unroll
    for (int e = CN(0); e < 24; ++e) ISSUE_C(e, 32);
    SBAR();
    v4u af[3];
#pragma unroll
    for (int t = 0; t < 3; ++t) af[t] = *(const LAS v4u*)(L + L_HF + (w * 3 + t) * 1024 + lane * 16);
#pragma unroll
    for (int u = 0; u < 4; ++u) wt[u] = f32x2{1.f, 1.f};
#pragma unroll
    for (int j = 0; j < 4; ++j) ca[j] = f32x4{0.f, 0.f, 0.f, 0.f};
#pragma unroll
    for (int t = 0; t < 3; ++t)
#pragma unroll
      for (int j = 0; j < 4; ++j) { v4u f; CFRAG(f, t, j); ca[j] = mfma(af[t], f, ca[j]); }
    st4<AUX_SC1>(tag, rws, w == 3 && lane == 0 ? 0 : WS_END, WS_PF + (cc * 28 + chunk) * 128);
    TS(8);
  }
#pragma unroll
  for (int j = 0; j < 4; ++j) *(LAS f32x4*)(L + L_RC + w * RCW + j * RCJ + lane * 16) = ca[j];
  LGKM0();
  __builtin_amdgcn_s_barrier();
  MEMFENCE();
  {
    const LAS unsigned char* q = L + L_RC + (lane & 3) * RCJ + (w >> 1) * 256 + (lane >> 2) * 16 + (w & 1) * 8;
    f32x2 t = {0.f, 0.f};
#pragma unroll
    for (int u = 0; u < 4; ++u)
#pragma unroll
      for (int s2 = 0; s2 < 2; ++s2) t += *(const LAS f32x2*)(q + u * RCW + s2 * 512) * wt[(u >> 1) * 2 + s2];
    st8<AUX_SC1>(v2u{tagf(t[0], tag), tagf(t[1], tag)}, rws, lane * 8, WS_PS + (cc * 4 + w) * (28 * 512) + chunk * 512);
  }
  TS(9);
  if (ii < 9 && w == 0) {
    const f32x2 gt = *(const LAS f32x2*)(L + L_GATE);
    const f32x4 g4 = {gt[0], gt[1], gt[0], gt[1]}, h4 = {1.f - gt[0], 1.f - gt[1], 1.f - gt[0], 1.f - gt[1]};
    const int pvo = WS_PS + (ii * 4 + (xcd >> 1)) * (28 * 512) + G * 512 + (xcd & 1) * 256 + n16 * 16;
    unsigned f = tag + 1;
#pragma nounroll
    for (unsigned n = 0; n < SPIN_G; ++n) {
      MEMFENCE();
      if (f != tag && lane < 28) f = __builtin_amdgcn_raw_buffer_load_b32(rws, lane * 128, WS_PF + ii * 28 * 128, AUX_SC1);
      if (__all(f == tag || lane >= 28)) break;
    }
    v4u pg[7];
#pragma nounroll
    for (unsigned n = 0; n < SPIN_G; ++n) {
      MEMFENCE();
#pragma unroll
      for (int i = 0; i < 7; ++i) pg[i] = ld16<AUX_SC1>(rws, pvo + (i & 1) * 2048, (i >> 1) * 4096);
      unsigned bad = 0;
#pragma unroll
      for (int i = 0; i < 7; ++i)
#pragma unroll
        for (int d = 0; d < 4; ++d) bad |= pg[i][d] ^ tag;
      if (__all((bad & 0xFFu) == 0)) break;
    }
    const f32x4 a4 = (__builtin_bit_cast(f32x4, pg[0]) + __builtin_bit_cast(f32x4, pg[1])) + (__builtin_bit_cast(f32x4, pg[2]) + __builtin_bit_cast(f32x4, pg[3]));
    const f32x4 b4 = (__builtin_bit_cast(f32x4, pg[4]) + __builtin_bit_cast(f32x4, pg[5])) + __builtin_bit_cast(f32x4, pg[6]);
    const f32x4 r4 = g4 * a4 + h4 * b4;
    const float res = swap16_sum(swap32_sum(r4[0], r4[1]), swap32_sum(r4[2], r4[3]));
    const unsigned ob = cvtpk(res, res);
    ((GAS unsigned short*)p.out)[((xcd >> 1) * 2 + (lane >> 5)) * DM + ii * 64 + (xcd & 1) * 32 + n16 * 2 + (G & 1)] = (unsigned short)ob;
    if (wg == 0 && lane == 0) st4<AUX_SC1>(tag, rws, 0, WS_E);
    TS(10);
  }
  TSOUT();
}
}

extern "C" void kernel_launch(void* const* d_in, const int* in_sizes, int n_in, void* d_out, int out_size, void* d_ws, size_t ws_size,
                              hipStream_t stream) {
  Params p;
  p.x = d_in[0]; p.kc = d_in[1]; p.vc = d_in[2]; p.wq = d_in[3]; p.wo = d_in[4];
  p.wg = d_in[6]; p.wu = d_in[7]; p.wd = d_in[8]; p.gw = d_in[9];
  p.out = d_out; p.ws = d_ws;
  fused_kernel<<<256, 512, 0, stream>>>(p);
}
```

```cpp
#include <hip/hip_runtime.h>
#include <hip/hip_bf16.h>
#include <stdint.h>

#define STAMP 0
#define WK 6
#define WQ 6
#define WV 6
#define WS_Q 10
#define WS_G 8
#define CSTEP 3
#define XPOS 20
#define CODE_BYTES 18560

namespace {
constexpr int NB = 8, DM = 576, HKV = 16, SK = 4096, DH = 128, WIN = 1024;
typedef unsigned v4u __attribute__((ext_vector_type(4)));
typedef unsigned v2u __attribute__((ext_vector_type(2)));
typedef float f32x4 __attribute__((ext_vector_type(4)));
typedef float f32x2 __attribute__((ext_vector_type(2)));
typedef short bf16x8 __attribute__((ext_vector_type(8)));
typedef __bf16 bf16x2 __attribute__((ext_vector_type(2)));
#define LAS __attribute__((address_space(3)))
#define GAS __attribute__((address_space(1)))
typedef __amdgpu_buffer_rsrc_t rsrc_t;
constexpr int AUX_NT = 2, AUX_SC1 = 16, RS3 = 0x00020000;
constexpr unsigned SENT = 0xFFFFFFFFu;

constexpr int WS_E = 0;
constexpr int WS_FL = 4096;
constexpr int WS_LS = WS_FL + 256 * 128;
constexpr int WS_QG = WS_LS + 256 * 128;
constexpr int WS_GU = WS_QG + 64 * 2048;
constexpr int WS_OS = WS_GU + 144 * 1024;
constexpr int WS_PS = WS_OS + 256 * 512;
constexpr int WS_END = WS_PS + 36 * 28 * 512;
constexpr int WS_TS = 8 << 20;

constexpr int L_TAG = 0, L_CNTA = 4, L_CNTML = 8, L_FQ = 12, L_FT = 16, L_GATE = 24;
constexpr int L_ML = 64;
constexpr int L_QF = 256;
constexpr int ORW = 8 * 36 * 4;
constexpr int L_OR = L_QF + 1024;
constexpr int SLOT = 576 * 4;
constexpr int L_RA = L_OR + 4 * ORW;
constexpr int RCJ = 1024 + 128, RCW = 4 * RCJ;
constexpr int L_RC = L_RA + 6 * SLOT;
constexpr int L_HF = L_RC + 4 * RCW;
constexpr int L_END = L_HF + 8192;

struct Params {
  const void* x; const void* kc; const void* vc; const void* wq; const void* wo; const void* wg; const void* wu; const void* wd; const void* gw;
  void* out; void* ws;
};

__device__ __forceinline__ rsrc_t mk(const void* p, unsigned bytes) { return __builtin_amdgcn_make_buffer_rsrc((void*)p, 0, (int)bytes, RS3); }
template <int AUX> __device__ __forceinline__ v4u ld16(rsrc_t r, int vo, int so) { return __builtin_amdgcn_raw_buffer_load_b128(r, vo, so, AUX); }
template <int AUX> __device__ __forceinline__ v2u ld8(rsrc_t r, int vo, int so) { return __builtin_amdgcn_raw_buffer_load_b64(r, vo, so, AUX); }
__device__ __forceinline__ void st16(v4u v, rsrc_t r, int vo) { __builtin_amdgcn_raw_buffer_store_b128(v, r, vo, 0, AUX_SC1); }
__device__ __forceinline__ void st8(v2u v, rsrc_t r, int vo, int so) { __builtin_amdgcn_raw_buffer_store_b64(v, r, vo, so, AUX_SC1); }
__device__ __forceinline__ void st4(unsigned v, rsrc_t r, int vo, int so) { __builtin_amdgcn_raw_buffer_store_b32(v, r, vo, so, AUX_SC1); }
__device__ __forceinline__ f32x4 mfma(v4u a, v4u b, f32x4 c) {
  return __builtin_amdgcn_mfma_f32_16x16x32_bf16(__builtin_bit_cast(bf16x8, a), __builtin_bit_cast(bf16x8, b), c, 0, 0, 0);
}
__device__ __forceinline__ unsigned cvtpk(float lo, float hi) {
  f32x2 v = {lo, hi};
  return __builtin_bit_cast(unsigned, __builtin_convertvector(v, bf16x2));
}
__device__ __forceinline__ unsigned nosent(unsigned u) { return u < 0xFFFFFFFEu ? u : 0xFFFFFFFEu; }
__device__ __forceinline__ unsigned umax3(unsigned a, unsigned b, unsigned c) { return max(max(a, b), c); }
__device__ __forceinline__ unsigned umax4(v4u v) { return max(umax3(v[0], v[1], v[2]), v[3]); }
__device__ __forceinline__ float dot2(unsigned a, unsigned b, float c) {
  return __builtin_amdgcn_fdot2_f32_bf16(__builtin_bit_cast(bf16x2, a), __builtin_bit_cast(bf16x2, b), c, false);
}
__device__ __forceinline__ float bflo(unsigned u) { return __builtin_bit_cast(float, u << 16); }
__device__ __forceinline__ float bfhi(unsigned u) { return __builtin_bit_cast(float, u & 0xffff0000u); }
template <int CTRL> __device__ __forceinline__ float dpp(float x) {
  return __builtin_bit_cast(float, __builtin_amdgcn_mov_dpp(__builtin_bit_cast(int, x), CTRL, 0xf, 0xf, true));
}
template <int CTRL> __device__ __forceinline__ unsigned dppu(unsigned x) { return (unsigned)__builtin_amdgcn_mov_dpp((int)x, CTRL, 0xf, 0xf, true); }
__device__ __forceinline__ float xrow_max(float x) {
  auto s = __builtin_amdgcn_permlane16_swap(__float_as_uint(x), __float_as_uint(x), false, false);
  x = fmaxf(__uint_as_float(s[0]), __uint_as_float(s[1]));
  auto t = __builtin_amdgcn_permlane32_swap(__float_as_uint(x), __float_as_uint(x), false, false);
  return fmaxf(__uint_as_float(t[0]), __uint_as_float(t[1]));
}
__device__ __forceinline__ float swap32_sum(float a, float b) {
  auto t = __builtin_amdgcn_permlane32_swap(__float_as_uint(a), __float_as_uint(b), false, false);
  return __uint_as_float(t[0]) + __uint_as_float(t[1]);
}
__device__ __forceinline__ float swap16_sum(float a, float b) {
  auto t = __builtin_amdgcn_permlane16_swap(__float_as_uint(a), __float_as_uint(b), false, false);
  return __uint_as_float(t[0]) + __uint_as_float(t[1]);
}
__device__ __forceinline__ float xrow_sum(float x) { x = swap16_sum(x, x); return swap32_sum(x, x); }
__device__ __forceinline__ float sigm(float x) { return __builtin_amdgcn_rcpf(1.f + __builtin_amdgcn_exp2f(-1.4426950408889634f * x)); }
#define SBAR() __builtin_amdgcn_sched_barrier(0)
#define MEMFENCE() asm volatile("" ::: "memory")
#define LGKM0() asm volatile("s_waitcnt lgkmcnt(0)" ::: "memory")
constexpr int vmc(int n) { return (n & 15) | (7 << 4) | (15 << 8) | ((n >> 4) << 14); }
#define VMWAIT(n) __builtin_amdgcn_s_waitcnt(vmc(n))

constexpr unsigned SPIN_G = 1u << 16;
constexpr unsigned SPIN_L = 1u << 22;

__device__ __forceinline__ unsigned lds_ld(LAS unsigned char* L, int off) { return __hip_atomic_load((LAS unsigned*)(L + off), __ATOMIC_RELAXED, __HIP_MEMORY_SCOPE_WORKGROUP); }
__device__ __forceinline__ void lds_wait_ge(LAS unsigned char* L, int off, unsigned want) {
#pragma nounroll
  for (unsigned n = 0; n < SPIN_L; ++n) {
    if ((unsigned)__builtin_amdgcn_readfirstlane(lds_ld(L, off)) >= want) break;
    __builtin_amdgcn_s_sleep(1);
  }
  MEMFENCE();
}
__device__ __forceinline__ unsigned lds_arrive(LAS unsigned char* L, int off, int lane) {
  LGKM0();
  unsigned old = 0;
  if (lane == 0) old = __hip_atomic_fetch_add((LAS unsigned*)(L + off), 1u, __ATOMIC_RELAXED, __HIP_MEMORY_SCOPE_WORKGROUP);
  old = __builtin_amdgcn_readfirstlane(old);
  MEMFENCE();
  return old;
}
__device__ __forceinline__ unsigned plo(unsigned hi_row, unsigned lo_row) { return __builtin_amdgcn_perm(hi_row, lo_row, 0x05040100u); }
__device__ __forceinline__ unsigned phi(unsigned hi_row, unsigned lo_row) { return __builtin_amdgcn_perm(hi_row, lo_row, 0x07060302u); }
template <int J0, int J1> __device__ __forceinline__ void tile_mma(const v4u (&r)[8], v4u a, f32x4 (&acc)[8]) {
#pragma unroll
  for (int j = J0; j < J1; ++j) {
    v4u bfr;
#pragma unroll
    for (int m = 0; m < 4; ++m) bfr[m] = (j & 1) ? phi(r[2 * m + 1][j >> 1], r[2 * m][j >> 1]) : plo(r[2 * m + 1][j >> 1], r[2 * m][j >> 1]);
    acc[j] = mfma(a, bfr, acc[j]);
  }
}

#define TS(i)
#define TSDECL
#define TSOUT()

__device__ __forceinline__ int gu_owner(int t, int u) { const int sa = 8 + 9 * u + (t >> 3), g = sa >= 13; return (t & 7) + 8 * (g * 16 + sa - 13 * g); }
__device__ __forceinline__ unsigned hpair(unsigned gp, unsigned up) {
  const float g0 = bflo(gp), g1 = bfhi(gp);
  return cvtpk(g0 * sigm(g0) * bflo(up), g1 * sigm(g1) * bfhi(up));
}

__global__ void __launch_bounds__(512, 2) fused_kernel(Params p) {
  __shared__ __attribute__((aligned(16))) unsigned char smem[L_END];
  LAS unsigned char* L = (LAS unsigned char*)smem;
  unsigned long long pc0;
  asm volatile("s_getpc_b64 %0" : "=s"(pc0));
  const int tid = threadIdx.x, lane = tid & 63, wv = __builtin_amdgcn_readfirstlane(tid >> 6);
  const int wg = blockIdx.x, xcd = wg & 7, ii = wg >> 3, grp = ii >> 4, mem = ii & 15;
  const int kvh = 2 * xcd + grp;
  const int G = lane >> 4, n16 = lane & 15;
  const bool isE = mem >= 13;
  const int sa = grp * 13 + mem;
  const int colblk = xcd + 8 * (sa < 8 ? sa : sa < 17 ? sa - 8 : sa - 17);
  const int du = (mem - (isE ? 13 : 9)) * 16 + kvh;
  const int dq = (du * 57) >> 9, dc = du - 9 * dq;
  TSDECL;
  TS(0);

  if (__builtin_expect(wv < 3, 1)) {
    const bool isQ = !isE && sa < 8;
    const char* wbase = isE ? (const char*)p.wd + (size_t)dq * (576 * DM * 2) + dc * 128 : (const char*)(isQ ? p.wq : sa < 17 ? p.wg : p.wu) + colblk * 128;
    const int rs = isE ? DM * 2 : isQ ? 8192 : 9216;
    const rsrc_t rw = mk(wbase, isE && du >= 45 ? 0 : DM * rs);
    const rsrc_t rx = mk(p.x, isE ? 0 : NB * DM * 2);
    const int vo = ((n16 >> 3) * 32 + G * 8) * rs + (n16 & 7) * 16;
    int so = wv * 192 * rs;
    v4u r[3][8], xa[3];
#pragma unroll
    for (int i = 0; i < 24; ++i) {
      if (i >= WS_Q) VMWAIT(WS_Q - 1);
      if (WS_G < WS_Q && i >= WS_G && !isQ) VMWAIT(WS_G - 1);
      r[i >> 3][i & 7] = ld16<AUX_NT>(rw, vo, so);
      so += (i & 7) == 7 ? 57 * rs : rs;
      if (i == XPOS) {
#pragma unroll
        for (int u = 0; u < 3; ++u) xa[u] = ld16<0>(rx, (n16 & 7) * (DM * 2) + ((n16 >> 3) * 32 + G * 8 + u * 64) * 2, wv * 192 * 2);
      }
      SBAR();
    }
    __builtin_amdgcn_s_barrier();
    SBAR();
    TS(1);
    const rsrc_t rws = mk(p.ws, WS_END);
    if (isE) {
      if (du >= 45) { TSOUT(); return; }
      lds_wait_ge(L, L_FT, 2);
      const int gvo = WS_GU + (dq * 9 + wv * 3) * 1024 + (n16 & 7) * 128 + (n16 >> 3) * 64 + G * 16;
      v4u gg[3], uu[3];
#pragma nounroll
      for (unsigned n = 0; n < SPIN_G; ++n) {
        MEMFENCE();
        unsigned mx = 0;
#pragma unroll
        for (int t = 0; t < 3; ++t) { gg[t] = ld16<AUX_SC1>(rws, gvo + t * 1024, 0); uu[t] = ld16<AUX_SC1>(rws, gvo + t * 1024, 72 * 1024); }
#pragma unroll
        for (int t = 0; t < 3; ++t) mx = umax3(mx, umax4(gg[t]), umax4(uu[t]));
        if (__all(mx != SENT)) break;
        __builtin_amdgcn_s_sleep(16);
      }
#pragma unroll
      for (int t = 0; t < 3; ++t)
#pragma unroll
        for (int d = 0; d < 4; ++d) xa[t][d] = hpair(gg[t][d], uu[t][d]);
      TS(4);
    }
    f32x4 acc[8];
#pragma unroll
    for (int j = 0; j < 8; ++j) acc[j] = f32x4{0.f, 0.f, 0.f, 0.f};
#pragma unroll
    for (int t = 0; t < 3; ++t) tile_mma<0, 8>(r[t], xa[t], acc);
    if ((G >> 1) == (n16 >> 3)) {
      LAS unsigned char* q = L + L_RA + (wv * 2 + (n16 >> 3)) * SLOT + ((G & 1) * 4 * 32 + (n16 & 7) * 4) * 4;
#pragma unroll
      for (int rr = 0; rr < 4; ++rr) {
        *(LAS f32x4*)(q + rr * 128) = f32x4{acc[0][rr], acc[1][rr], acc[2][rr], acc[3][rr]};
        *(LAS f32x4*)(q + rr * 128 + 288 * 4) = f32x4{acc[4][rr], acc[5][rr], acc[6][rr], acc[7][rr]};
      }
    }
    TS(2);
    if (lds_arrive(L, L_CNTA, lane) == 2) {
      if (isE) {
        f32x4 s0 = {0.f, 0.f, 0.f, 0.f}, s1 = {0.f, 0.f, 0.f, 0.f};
        const LAS unsigned char* q = L + L_RA + ((n16 & 1) * 288 + G * 64 + (n16 >> 1) * 4) * 4;
#pragma unroll
        for (int sl = 0; sl < 6; ++sl) { s0 += *(const LAS f32x4*)(q + sl * SLOT); s1 += *(const LAS f32x4*)(q + sl * SLOT + 128); }
        const int o = WS_PS + (dc * 4 + G) * (28 * 512) + (16 + dq) * 512 + n16 * 32;
        st16(v4u{nosent(__float_as_uint(s0[0])), nosent(__float_as_uint(s1[0])), nosent(__float_as_uint(s0[1])), nosent(__float_as_uint(s1[1]))}, rws, o);
        st16(v4u{nosent(__float_as_uint(s0[2])), nosent(__float_as_uint(s1[2])), nosent(__float_as_uint(s0[3])), nosent(__float_as_uint(s1[3]))}, rws, o + 16);
      } else {
        f32x4 s0 = {0.f, 0.f, 0.f, 0.f}, s1 = {0.f, 0.f, 0.f, 0.f};
#pragma unroll
        for (int sl = 0; sl < 6; ++sl) {
          s0 += *(LAS f32x4*)(L + L_RA + sl * SLOT + lane * 16);
          s1 += *(LAS f32x4*)(L + L_RA + sl * SLOT + lane * 16 + 288 * 4);
        }
        const unsigned c0 = cvtpk(s0[0], s0[1]), c1 = cvtpk(s0[2], s0[3]), c2 = cvtpk(s1[0], s1[1]), c3 = cvtpk(s1[2], s1[3]);
        if (isQ) {
          const unsigned tag = lds_ld(L, L_TAG);
          const int o = WS_QG + colblk * 2048 + (lane >> 3) * 256 + (lane & 7) * 16;
          st16(v4u{c0, tag, c1, tag}, rws, o);
          st16(v4u{c2, tag, c3, tag}, rws, o + 128);
        } else {
          lds_wait_ge(L, L_FT, 1);
          st16(v4u{nosent(c0), nosent(c1), nosent(c2), nosent(c3)}, rws, WS_GU + (colblk + (sa < 17 ? 0 : 72)) * 1024 + lane * 16);
        }
      }
      TS(3);
    }
    TSOUT();
    return;
  }

  const rsrc_t rws = mk(p.ws, WS_END);
  const bool isO = mem < 9, isD = !isO && !isE && du < 63;
  const int cc = isO ? mem : dc, chunk = isO ? kvh : (isE ? 16 : 21) + dq;
  const bool hasC = isO || isD;

  if (wv == 3) {
    const unsigned e0 = __builtin_amdgcn_raw_buffer_load_b32(rws, 0, WS_E, AUX_SC1);
    const v4u cp = ld16<0>(mk((const void*)(pc0 & ~127ull), CODE_BYTES), lane * 16, ii * 1024);
    if (lane < 4) *(LAS unsigned*)(L + 4 + lane * 4) = 0u;
    const unsigned tag = __builtin_amdgcn_readfirstlane(e0) + 1u;
    if (lane == 0) *(LAS unsigned*)(L + L_TAG) = tag;
    LGKM0();
    __builtin_amdgcn_s_barrier();
    {
      const v4u ss = {SENT, SENT, SENT, SENT};
      st8(v2u{SENT, SENT}, rws, lane * 8, WS_OS + (kvh * 16 + mem) * 512);
      if (lane < 8) st16(ss, rws, WS_LS + (kvh * 16 + mem) * 128 + lane * 16);
      if (hasC || (isE && du < 45)) {
        const int o = WS_PS + (cc * 4 + (lane >> 5)) * (28 * 512) + chunk * 512 + (lane & 31) * 16;
        st16(ss, rws, o);
        st16(ss, rws, o + 2 * 28 * 512);
      }
      if (!isE && sa >= 8) st16(ss, rws, WS_GU + (colblk + (sa < 17 ? 0 : 72)) * 1024 + lane * 16);
    }
    asm volatile("s_waitcnt vmcnt(0)" ::: "memory");
    asm volatile("" ::"v"(cp));
    if (lane < 8) st16(v4u{tag, tag, tag, tag}, rws, WS_FL + wg * 128 + lane * 16);
    lds_arrive(L, L_FT, lane);
    TS(1);
    if (ii < 9) {
      const int rb = (xcd >> 1) * 2 + (lane >> 5), l32 = lane & 31;
      const rsrc_t rx = mk(p.x, NB * DM * 2), rg = mk(p.gw, DM * 2);
      float gs = 0.f;
      v4u xv[3], gv[3];
#pragma unroll
      for (int u = 0; u < 3; ++u) {
        xv[u] = ld16<0>(rx, (u == 2 && l32 >= 8 ? NB : rb) * (DM * 2) + l32 * 16 + u * 512, 0);
        gv[u] = ld16<0>(rg, l32 * 16 + u * 512, 0);
      }
#pragma unroll
      for (int u = 0; u < 3; ++u)
#pragma unroll
        for (int d = 0; d < 4; ++d) gs = dot2(xv[u][d], gv[u][d], gs);
      gs += dpp<0xB1>(gs); gs += dpp<0x4E>(gs); gs += dpp<0x141>(gs); gs += dpp<0x128>(gs);
      gs = swap16_sum(gs, gs);
      if (l32 == 0) *(LAS float*)(L + L_GATE + (lane >> 5) * 4) = sigm(gs);
    }
    {
      const int c16 = 4 * G + (n16 & 3), h = (n16 >> 2) & 1;
      const int vo = (kvh * 4 + 2 * h + (c16 >> 3)) * 2048 + (mem >> 1) * 256 + (c16 & 7) * 16;
      v4u g0, g1;
#pragma nounroll
      for (unsigned n = 0; n < SPIN_G; ++n) {
        MEMFENCE();
        g0 = ld16<AUX_SC1>(rws, vo, WS_QG);
        g1 = ld16<AUX_SC1>(rws, vo + 128, WS_QG);
        const bool ok = g0[1] == tag && g0[3] == tag && g1[1] == tag && g1[3] == tag;
        if (__all(ok)) break;
        __builtin_amdgcn_s_sleep(4);
      }
      *(LAS v4u*)(L + L_QF + lane * 16) = v4u{g0[0], g0[2], g1[0], g1[2]};
      lds_arrive(L, L_FQ, lane);
      TS(2);
    }
    {
      int src;
      if (isO) {
        const int c = lane - 16, d = c - 16, u = (d < 5 ? d : d - 5) * 9 + ii, k2 = c < 16 ? c : u & 15;
        const int m2 = c < 16 ? ii : (d < 5 ? 13 : 9) + (u >> 4);
        src = lane < 16 || ii >= 9 || lane >= 44 ? xcd + 8 * (grp * 16 + n16) : (k2 >> 1) + 8 * ((k2 & 1) * 16 + m2);
      } else {
        const int t = min(isE ? dq * 9 + min(n16, 8) : 45 + dq * 4 + (n16 & 3), 71);
        src = gu_owner(t, G & 1);
      }
#pragma nounroll
      for (unsigned n = 0; n < SPIN_G; ++n) {
        MEMFENCE();
        const unsigned f = __builtin_amdgcn_raw_buffer_load_b32(rws, src * 128, WS_FL, AUX_SC1);
        if (__all(f == tag)) break;
        __builtin_amdgcn_s_sleep(8);
      }
      lds_arrive(L, L_FT, lane);
      TS(3);
    }
    if (isD) {
      const bool up = n16 >= 8;
      const int gvo = WS_GU + (up ? 72 * 1024 : 0) + (n16 & 7) * 128 + G * 16;
      v4u ag[8];
#pragma nounroll
      for (unsigned n = 0; n < SPIN_G; ++n) {
        MEMFENCE();
        unsigned mx = 0;
#pragma unroll
        for (int t = 0; t < 8; ++t) ag[t] = ld16<AUX_SC1>(rws, gvo + (t & 1) * 64, min(45 + dq * 4 + (t >> 1), 71) * 1024);
#pragma unroll
        for (int t = 0; t < 8; ++t) mx = max(mx, umax4(ag[t]));
        if (__all(mx != SENT)) break;
        __builtin_amdgcn_s_sleep(16);
      }
#pragma unroll
      for (int t = 0; t < 8; ++t) {
        unsigned hp[2];
#pragma unroll
        for (int d = 0; d < 2; ++d) {
          const unsigned send = up ? ag[t][d] : ag[t][2 + d], keep = up ? ag[t][2 + d] : ag[t][d];
          const unsigned recv = dppu<0x128>(send);
          hp[d] = hpair(up ? recv : keep, up ? keep : recv);
        }
        *(LAS v4u*)(L + L_HF + t * 1024 + lane * 16) = v4u{up ? 0u : hp[0], up ? 0u : hp[1], up ? hp[0] : 0u, up ? hp[1] : 0u};
      }
      LGKM0();
      TS(4);
    }
    TSOUT();
    return;
  }

  {
    const int w = wv - 4, b = mem >> 1, sp = mem & 1;
    const size_t kvoff = ((size_t)(b * HKV + kvh) * SK + (SK - WIN) + sp * 512) * (DH * 2) + w * 1024;
    const rsrc_t rk = mk((const char*)p.kc + kvoff, 131072 - w * 1024), rv = mk((const char*)p.vc + kvoff, 131072 - w * 1024);
    const int kvo = (n16 >> 2) * 256 + (G * 4 + (n16 & 3)) * 16, vvo = lane * 16;
    const char* cb = isO ? (const char*)p.wo + (size_t)(kvh * 256 + w * 64) * (DM * 2) : (const char*)p.wd + (size_t)(2880 + dq * 256 + w * 64) * (DM * 2);
    const rsrc_t rc = mk(cb + cc * 128, isO || (isD && dq * 4 + w < 27) ? 64 * DM * 2 - cc * 128 : 0);
    const int cvo = G * 8 * (DM * 2) + n16 * 8;
    __builtin_amdgcn_s_barrier();
    SBAR();
    TS(1);
    v4u K[32], V[4][8];
#define ISSUE_K(j) do { if ((j) >= WK) VMWAIT(WK - 1); K[j] = ld16<AUX_NT>(rk, kvo, (j) * 4096); SBAR(); } while (0)
#define ISSUE_V(j) do { SBAR(); if ((j) < 3) VMWAIT(WK - 1); else if ((j) < 23) VMWAIT(WQ - 1); else VMWAIT(WV - 1); V[(j) >> 3][(j) & 7] = ld16<AUX_NT>(rv, vvo, (j) * 4096); SBAR(); } while (0)
#pragma unroll
    for (int j = 0; j < 32; ++j) ISSUE_K(j);
#pragma unroll
    for (int j = 0; j < 4; ++j) ISSUE_V(j);
    TS(2);
    lds_wait_ge(L, L_FQ, 1);
    TS(3);
    const v4u qf = *(LAS v4u*)(L + L_QF + lane * 16);
    float s[32];
#pragma unroll
    for (int j = 0; j < 32; ++j) {
      if ((j & 3) == 0) ISSUE_V(4 + (j >> 2));
      const f32x4 d = mfma(K[j], qf, f32x4{0.f, 0.f, 0.f, 0.f});
      s[j] = dpp<0xFF>(d[3]) + (dpp<0xAA>(d[2]) + (dpp<0x55>(d[1]) + d[0]));
    }
    ISSUE_V(12);
    float m = s[0];
#pragma unroll
    for (int j = 1; j < 32; ++j) m = fmaxf(m, s[j]);
    m = xrow_max(m);
    constexpr float C2 = 0.08838834764831845f * 1.4426950408889634f;
    const float mc = m * C2;
    float l = 0.f;
    v4u pf[4];
#pragma unroll
    for (int j = 0; j < 32; j += 2) {
      if ((j & 7) == 0) ISSUE_V(13 + (j >> 3));
      const float p0 = __builtin_amdgcn_exp2f(fmaf(s[j], C2, -mc)), p1 = __builtin_amdgcn_exp2f(fmaf(s[j + 1], C2, -mc));
      l += p0 + p1;
      pf[j >> 3][(j & 7) >> 1] = cvtpk(p0, p1);
    }
    ISSUE_V(17);
    l = xrow_sum(l);
    if (lane == 0 || lane == 4) *(LAS f32x2*)(L + L_ML + (w * 2 + (lane >> 2)) * 8) = f32x2{mc, l};
    LGKM0();
    if (lane == 0) __hip_atomic_fetch_add((LAS unsigned*)(L + L_CNTML), 1u, __ATOMIC_RELAXED, __HIP_MEMORY_SCOPE_WORKGROUP);
    TS(4);
    f32x4 acc[8];
#pragma unroll
    for (int j = 0; j < 8; ++j) acc[j] = f32x4{0.f, 0.f, 0.f, 0.f};
    ISSUE_V(18); tile_mma<0, 4>(V[0], pf[0], acc);
    ISSUE_V(19); tile_mma<4, 8>(V[0], pf[0], acc);
    ISSUE_V(20);
    ISSUE_V(21); tile_mma<0, 4>(V[1], pf[1], acc);
    ISSUE_V(22); tile_mma<4, 8>(V[1], pf[1], acc);
    ISSUE_V(23);
    ISSUE_V(24);
    lds_wait_ge(L, L_CNTML, 4);
    float fw[4];
    {
      f32x2 ml[4];
#pragma unroll
      for (int u = 0; u < 4; ++u) ml[u] = *(LAS f32x2*)(L + L_ML + (u * 2 + ((w == 2 ? lane : w) & 1)) * 8);
      const float M = fmaxf(fmaxf(ml[0][0], ml[1][0]), fmaxf(ml[2][0], ml[3][0]));
      float Ls = 0.f;
#pragma unroll
      for (int u = 0; u < 4; ++u) { fw[u] = __builtin_amdgcn_exp2f(ml[u][0] - M); Ls = fmaf(ml[u][1], fw[u], Ls); }
      const float inv = __builtin_amdgcn_rcpf(Ls);
#pragma unroll
      for (int u = 0; u < 4; ++u) fw[u] *= inv;
      const float lse = M + __builtin_amdgcn_logf(Ls);
      if (w == 2 && lane < 2) st4(nosent(__float_as_uint(lse)), rws, lane * 4, WS_LS + (kvh * 16 + mem) * 128);
    }
    ISSUE_V(25);
    ISSUE_V(26);
    ISSUE_V(27);
    ISSUE_V(28);
    ISSUE_V(29); tile_mma<0, 4>(V[2], pf[2], acc);
    ISSUE_V(30); tile_mma<4, 8>(V[2], pf[2], acc);
    ISSUE_V(31);
    TS(5);
    v2u c[16];
    v4u bl[8];
#define ISSUE_C(e) c[e] = ld8<AUX_NT>(rc, cvo + ((e) & 3) * (DM * 2), (((e) >> 3) * 32 + ((e) & 4)) * (DM * 2))
#define PERM_V3(mm) _Pragma("unroll") for (int j = 0; j < 8; ++j) bl[j][mm] = (j & 1) ? phi(V[3][2 * (mm) + 1][j >> 1], V[3][2 * (mm)][j >> 1]) : plo(V[3][2 * (mm) + 1][j >> 1], V[3][2 * (mm)][j >> 1])
#define CSTEP_AFTER(vleft, st) do { SBAR(); VMWAIT((vleft) + (st) * CSTEP); _Pragma("unroll") for (int e_ = (st) * CSTEP; e_ < ((st) + 1) * CSTEP; ++e_) ISSUE_C(e_); SBAR(); } while (0)
    CSTEP_AFTER(4, 0); PERM_V3(0); PERM_V3(1);
    CSTEP_AFTER(3, 1);
    CSTEP_AFTER(2, 2); PERM_V3(2);
    CSTEP_AFTER(1, 3);
#pragma unroll
    for (int j = 0; j < 8; ++j) {
      bl[j][3] = (j & 1) ? phi(V[3][7][j >> 1], V[3][6][j >> 1]) : plo(V[3][7][j >> 1], V[3][6][j >> 1]);
      acc[j] = mfma(pf[3], bl[j], acc[j]);
    }
    TS(6);
    if (lane < 32) {
      LAS float* q = (LAS float*)(L + L_OR + w * ORW) + lane;
#pragma unroll
      for (int j = 0; j < 8; ++j) q[j * 36] = acc[j][0];
    }
    LGKM0();
    __builtin_amdgcn_s_barrier();
    MEMFENCE();
    if (w < 2) {
      const LAS float* q = (const LAS float*)(L + L_OR) + (lane & 3) * 72 + w * 16 + (lane >> 2);
      float o0 = 0.f, o1 = 0.f;
#pragma unroll
      for (int u = 0; u < 4; ++u) { o0 = fmaf(q[u * 288], fw[u], o0); o1 = fmaf(q[u * 288 + 36], fw[u], o1); }
      st4(nosent(cvtpk(o0, o1)), rws, lane * 4, WS_OS + (kvh * 16 + mem) * 512 + w * 256);
    }
    TS(7);
    if (!hasC) { TSOUT(); return; }
    f32x4 ca[4];
#pragma unroll
    for (int j = 0; j < 4; ++j) ca[j] = f32x4{0.f, 0.f, 0.f, 0.f};
    f32x2 wt[4];
    auto cfrag = [&](int ks, int j) { v4u f;
#pragma unroll
      for (int mm = 0; mm < 4; ++mm) f[mm] = (j & 1) ? phi(c[ks * 8 + 2 * mm + 1][j >> 1], c[ks * 8 + 2 * mm][j >> 1]) : plo(c[ks * 8 + 2 * mm + 1][j >> 1], c[ks * 8 + 2 * mm][j >> 1]);
      return f; };
#pragma unroll
    for (int e = 4 * CSTEP; e < 16; ++e) ISSUE_C(e);
    v4u a0, a1, bf[8];
    if (isO) {
      const int lvo = WS_LS + (kvh * 16 + w * 4 + (lane & 3)) * 128;
      v2u lg = ld8<AUX_SC1>(rws, lvo, 0);
      const int ovo = WS_OS + kvh * 8192 + ((n16 & 7) * 2 + (n16 >> 3)) * 512 + w * 128 + G * 16;
      a0 = ld16<AUX_SC1>(rws, ovo, 0); a1 = ld16<AUX_SC1>(rws, ovo + 64, 0);
      SBAR();
#pragma unroll
      for (int j = 0; j < 8; ++j) bf[j] = cfrag(j >> 2, j & 3);
      SBAR();
#pragma nounroll
      for (unsigned n = 0; n < SPIN_G; ++n) {
        if (__all(max(lg[0], lg[1]) != SENT)) break;
        MEMFENCE();
        lg = ld8<AUX_SC1>(rws, lvo, 0);
      }
#pragma unroll
      for (int h = 0; h < 2; ++h) {
        const float own = __uint_as_float(lg[h]);
        const float wo_ = __builtin_amdgcn_rcpf(1.f + __builtin_amdgcn_exp2f(dpp<0xB1>(own) - own));
        wt[h * 2] = f32x2{dpp<0x00>(wo_), dpp<0xAA>(wo_)};
        wt[h * 2 + 1] = f32x2{dpp<0x55>(wo_), dpp<0xFF>(wo_)};
      }
      SBAR();
#pragma nounroll
      for (unsigned n = 0; n < SPIN_G; ++n) {
        if (__all(max(umax4(a0), umax4(a1)) != SENT)) break;
        MEMFENCE();
        a0 = ld16<AUX_SC1>(rws, ovo, 0); a1 = ld16<AUX_SC1>(rws, ovo + 64, 0);
      }
    } else {
      a0 = *(const LAS v4u*)(L + L_HF + w * 2048 + lane * 16);
      a1 = *(const LAS v4u*)(L + L_HF + w * 2048 + 1024 + lane * 16);
#pragma unroll
      for (int u = 0; u < 4; ++u) wt[u] = f32x2{1.f, 1.f};
#pragma unroll
      for (int j = 0; j < 8; ++j) bf[j] = cfrag(j >> 2, j & 3);
    }
    TS(8);
#pragma unroll
    for (int j = 0; j < 4; ++j) ca[j] = mfma(a0, bf[j], ca[j]);
#pragma unroll
    for (int j = 0; j < 4; ++j) ca[j] = mfma(a1, bf[4 + j], ca[j]);
#pragma unroll
    for (int j = 0; j < 4; ++j) *(LAS f32x4*)(L + L_RC + w * RCW + j * RCJ + lane * 16) = ca[j];
    LGKM0();
    __builtin_amdgcn_s_barrier();
    MEMFENCE();
    {
      const LAS unsigned char* q = L + L_RC + (lane & 3) * RCJ + (w >> 1) * 256 + (lane >> 2) * 16 + (w & 1) * 8;
      f32x2 t = {0.f, 0.f};
#pragma unroll
      for (int u = 0; u < 4; ++u)
#pragma unroll
        for (int s2 = 0; s2 < 2; ++s2) t += *(const LAS f32x2*)(q + u * RCW + s2 * 512) * wt[(u >> 1) * 2 + s2];
      st8(v2u{nosent(__float_as_uint(t[0])), nosent(__float_as_uint(t[1]))}, rws, lane * 8, WS_PS + (cc * 4 + w) * (28 * 512) + chunk * 512);
    }
    TS(9);
    if (ii < 9 && w == 0) {
      const f32x2 gt = *(const LAS f32x2*)(L + L_GATE);
      const f32x4 g4 = {gt[0], gt[1], gt[0], gt[1]}, h4 = {1.f - gt[0], 1.f - gt[1], 1.f - gt[0], 1.f - gt[1]};
      const int pvo = WS_PS + (ii * 4 + (xcd >> 1)) * (28 * 512) + G * 512 + (xcd & 1) * 256 + n16 * 16;
      float res = 0.f;
#pragma nounroll
      for (unsigned n = 0; n < SPIN_G; ++n) {
        MEMFENCE();
        v4u pg[7];
#pragma unroll
        for (int i = 0; i < 7; ++i) pg[i] = ld16<AUX_SC1>(rws, pvo + (i & 1) * 2048, (i >> 1) * 4096);
        const f32x4 a4 = (__builtin_bit_cast(f32x4, pg[0]) + __builtin_bit_cast(f32x4, pg[1])) + (__builtin_bit_cast(f32x4, pg[2]) + __builtin_bit_cast(f32x4, pg[3]));
        const f32x4 b4 = (__builtin_bit_cast(f32x4, pg[4]) + __builtin_bit_cast(f32x4, pg[5])) + __builtin_bit_cast(f32x4, pg[6]);
        const f32x4 r4 = g4 * a4 + h4 * b4;
        res = swap16_sum(swap32_sum(r4[0], r4[1]), swap32_sum(r4[2], r4[3]));
        if (__all(res == res)) break;
        unsigned mx = 0;
#pragma unroll
        for (int i = 0; i < 7; ++i) mx = max(mx, umax4(pg[i]));
        if (__all(mx != SENT)) break;
      }
      const unsigned ob = cvtpk(res, res);
      ((GAS unsigned short*)p.out)[((xcd >> 1) * 2 + (lane >> 5)) * DM + ii * 64 + (xcd & 1) * 32 + n16 * 2 + (G & 1)] = (unsigned short)ob;
      if (wg == 0 && lane == 0) st4(lds_ld(L, L_TAG), rws, 0, WS_E);
      TS(10);
    }
    TSOUT();
  }
}
}

extern "C" void kernel_launch(void* const* d_in, const int* in_sizes, int n_in, void* d_out, int out_size, void* d_ws, size_t ws_size,
                              hipStream_t stream) {
  Params p;
  p.x = d_in[0]; p.kc = d_in[1]; p.vc = d_in[2]; p.wq = d_in[3]; p.wo = d_in[4];
  p.wg = d_in[6]; p.wu = d_in[7]; p.wd = d_in[8]; p.gw = d_in[9];
  p.out = d_out; p.ws = d_ws;
  fused_kernel<<<256, 512, 0, stream>>>(p);
}
```

```cpp
#include <hip/hip_runtime.h>
#include <hip/hip_bf16.h>
#include <stdint.h>

#define STAMP 0
#define WW 8
#define WLO 6
#define LAG 40
#define WS 11
#define XPOS 5
#define CSTEP 2
#define TV 1220
#define PBASE 12
#define CHK 4
#define QT0 600
#define GT0 800
#define LSEPOS 2
#define PSQ 256
#define PSG 256
#define PSO 256
#define PSP 256
#define FS 128
#define PRIO 2
#define CODE_BYTES 20864

namespace {
constexpr int NB = 8, DM = 576, HKV = 16, SK = 4096, DH = 128, WIN = 1024;
typedef unsigned v4u __attribute__((ext_vector_type(4)));
typedef unsigned v2u __attribute__((ext_vector_type(2)));
typedef float f32x4 __attribute__((ext_vector_type(4)));
typedef float f32x2 __attribute__((ext_vector_type(2)));
typedef short bf16x8 __attribute__((ext_vector_type(8)));
typedef __bf16 bf16x2 __attribute__((ext_vector_type(2)));
#define LAS __attribute__((address_space(3)))
#define GAS __attribute__((address_space(1)))
typedef __amdgpu_buffer_rsrc_t rsrc_t;
constexpr int AUX_NT = 2, AUX_SC1 = 16, RS3 = 0x00020000;

constexpr int WS_E = 0;
constexpr int WS_GF = 4096;
constexpr int WS_LS = WS_GF + 144 * FS;
constexpr int WS_PF = WS_LS + 256 * FS;
constexpr int WS_QG = WS_PF + 252 * FS;
constexpr int WS_GU = WS_QG + 512 * PSQ;
constexpr int WS_OG = WS_GU + 576 * PSG;
constexpr int WS_PS = WS_OG + 1024 * PSO;
constexpr int WS_END = WS_PS + 2016 * PSP;
#define WSMB 300
constexpr size_t WS_BASE = (size_t)WSMB << 20;
constexpr int WS_TS = 8 << 20;

constexpr int L_TAG = 0, L_CNTA = 4, L_CNTML = 8, L_FQ = 12, L_FG = 16, L_FT = 20, L_GATE = 24;
constexpr int L_ML = 64;
constexpr int L_QF = 256;
constexpr int ORW = 8 * 36 * 4;
constexpr int L_OR = L_QF + 1024;
constexpr int SLOT = 576 * 4;
constexpr int L_RA = L_OR + 4 * ORW;
constexpr int RCJ = 1024 + 128, RCW = 4 * RCJ;
constexpr int L_RC = L_RA + 6 * SLOT;
constexpr int L_HF = L_RC + 4 * RCW;
constexpr int L_END = L_HF + 12288;

struct Params {
  const void* x; const void* kc; const void* vc; const void* wq; const void* wo; const void* wg; const void* wu; const void* wd; const void* gw;
  void* out; void* ws;
};

__device__ __forceinline__ rsrc_t mk(const void* p, unsigned bytes) { return __builtin_amdgcn_make_buffer_rsrc((void*)p, 0, (int)bytes, RS3); }
template <int AUX> __device__ __forceinline__ v4u ld16(rsrc_t r, int vo, int so) { return __builtin_amdgcn_raw_buffer_load_b128(r, vo, so, AUX); }
template <int AUX> __device__ __forceinline__ v2u ld8(rsrc_t r, int vo, int so) { return __builtin_amdgcn_raw_buffer_load_b64(r, vo, so, AUX); }
template <int AUX> __device__ __forceinline__ void st16(v4u v, rsrc_t r, int vo) { __builtin_amdgcn_raw_buffer_store_b128(v, r, vo, 0, AUX); }
template <int AUX> __device__ __forceinline__ void st8(v2u v, rsrc_t r, int vo, int so) { __builtin_amdgcn_raw_buffer_store_b64(v, r, vo, so, AUX); }
template <int AUX> __device__ __forceinline__ void st4(unsigned v, rsrc_t r, int vo, int so) { __builtin_amdgcn_raw_buffer_store_b32(v, r, vo, so, AUX); }
__device__ __forceinline__ f32x4 mfma(v4u a, v4u b, f32x4 c) {
  return __builtin_amdgcn_mfma_f32_16x16x32_bf16(__builtin_bit_cast(bf16x8, a), __builtin_bit_cast(bf16x8, b), c, 0, 0, 0);
}
__device__ __forceinline__ unsigned cvtpk(float lo, float hi) {
  f32x2 v = {lo, hi};
  return __builtin_bit_cast(unsigned, __builtin_convertvector(v, bf16x2));
}
__device__ __forceinline__ unsigned tagf(float v, unsigned tag) { return __builtin_amdgcn_perm(__float_as_uint(v), tag, 0x07060500u); }
__device__ __forceinline__ float dot2(unsigned a, unsigned b, float c) {
  return __builtin_amdgcn_fdot2_f32_bf16(__builtin_bit_cast(bf16x2, a), __builtin_bit_cast(bf16x2, b), c, false);
}
__device__ __forceinline__ float bflo(unsigned u) { return __builtin_bit_cast(float, u << 16); }
__device__ __forceinline__ float bfhi(unsigned u) { return __builtin_bit_cast(float, u & 0xffff0000u); }
template <int CTRL> __device__ __forceinline__ float dpp(float x) {
  return __builtin_bit_cast(float, __builtin_amdgcn_mov_dpp(__builtin_bit_cast(int, x), CTRL, 0xf, 0xf, true));
}
template <int CTRL> __device__ __forceinline__ unsigned dppu(unsigned x) { return (unsigned)__builtin_amdgcn_mov_dpp((int)x, CTRL, 0xf, 0xf, true); }
__device__ __forceinline__ float xrow_max(float x) {
  auto s = __builtin_amdgcn_permlane16_swap(__float_as_uint(x), __float_as_uint(x), false, false);
  x = fmaxf(__uint_as_float(s[0]), __uint_as_float(s[1]));
  auto t = __builtin_amdgcn_permlane32_swap(__float_as_uint(x), __float_as_uint(x), false, false);
  return fmaxf(__uint_as_float(t[0]), __uint_as_float(t[1]));
}
__device__ __forceinline__ float swap32_sum(float a, float b) {
  auto t = __builtin_amdgcn_permlane32_swap(__float_as_uint(a), __float_as_uint(b), false, false);
  return __uint_as_float(t[0]) + __uint_as_float(t[1]);
}
__device__ __forceinline__ float swap16_sum(float a, float b) {
  auto t = __builtin_amdgcn_permlane16_swap(__float_as_uint(a), __float_as_uint(b), false, false);
  return __uint_as_float(t[0]) + __uint_as_float(t[1]);
}
__device__ __forceinline__ float xrow_sum(float x) { x = swap16_sum(x, x); return swap32_sum(x, x); }
__device__ __forceinline__ float sigm(float x) { return __builtin_amdgcn_rcpf(1.f + __builtin_amdgcn_exp2f(-1.4426950408889634f * x)); }
#define SBAR() __builtin_amdgcn_sched_barrier(0)
#define MEMFENCE() asm volatile("" ::: "memory")
#define LGKM0() asm volatile("s_waitcnt lgkmcnt(0)" ::: "memory")
constexpr int vmc(int n) { return (n & 15) | (7 << 4) | (15 << 8) | ((n >> 4) << 14); }
#define VMWAIT(n) __builtin_amdgcn_s_waitcnt(vmc(n))
#define NOW() ((unsigned)__builtin_amdgcn_s_memrealtime())

constexpr unsigned SPIN_G = 1u << 16;
constexpr unsigned SPIN_L = 1u << 22;

__device__ __forceinline__ unsigned lds_ld(LAS unsigned char* L, int off) { return __hip_atomic_load((LAS unsigned*)(L + off), __ATOMIC_RELAXED, __HIP_MEMORY_SCOPE_WORKGROUP); }
template <int SL = 1> __device__ __forceinline__ void lds_wait_ge(LAS unsigned char* L, int off, unsigned want) {
#pragma nounroll
  for (unsigned n = 0; n < SPIN_L; ++n) {
    if ((unsigned)__builtin_amdgcn_readfirstlane(lds_ld(L, off)) >= want) break;
    __builtin_amdgcn_s_sleep(SL);
  }
  MEMFENCE();
}
__device__ __forceinline__ unsigned lds_arrive(LAS unsigned char* L, int off, int lane) {
  LGKM0();
  unsigned old = 0;
  if (lane == 0) old = __hip_atomic_fetch_add((LAS unsigned*)(L + off), 1u, __ATOMIC_RELAXED, __HIP_MEMORY_SCOPE_WORKGROUP);
  old = __builtin_amdgcn_readfirstlane(old);
  MEMFENCE();
  return old;
}
__device__ __forceinline__ unsigned plo(unsigned hi_row, unsigned lo_row) { return __builtin_amdgcn_perm(hi_row, lo_row, 0x05040100u); }
__device__ __forceinline__ unsigned phi(unsigned hi_row, unsigned lo_row) { return __builtin_amdgcn_perm(hi_row, lo_row, 0x07060302u); }
template <int J0, int J1> __device__ __forceinline__ void tile_mma(const v4u (&r)[8], v4u a, f32x4 (&acc)[8]) {
#pragma unroll
  for (int j = J0; j < J1; ++j) {
    v4u bfr;
#pragma unroll
    for (int m = 0; m < 4; ++m) bfr[m] = (j & 1) ? phi(r[2 * m + 1][j >> 1], r[2 * m][j >> 1]) : plo(r[2 * m + 1][j >> 1], r[2 * m][j >> 1]);
    acc[j] = mfma(a, bfr, acc[j]);
  }
}

#define TS(i)
#define TSDECL
#define TSOUT()

__device__ __forceinline__ unsigned hpair(unsigned gp, unsigned up) {
  const float g0 = bflo(gp), g1 = bfhi(gp);
  return cvtpk(g0 * sigm(g0) * bflo(up), g1 * sigm(g1) * bfhi(up));
}

__global__ void __launch_bounds__(512, 2) fused_kernel(Params p) {
  __shared__ __attribute__((aligned(16))) unsigned char smem[L_END + (STAMP ? 512 : 0)];
  LAS unsigned char* L = (LAS unsigned char*)smem;
  unsigned long long pc0;
  asm volatile("s_getpc_b64 %0" : "=s"(pc0));
  const unsigned t0 = (unsigned)__builtin_amdgcn_s_memrealtime();
  const int tid = threadIdx.x, lane = tid & 63, wv = __builtin_amdgcn_readfirstlane(tid >> 6);
  const int wg = blockIdx.x, xcd = wg & 7, ii = wg >> 3, grp = ii >> 4, mem = ii & 15;
  const int G = lane >> 4, n16 = lane & 15;
  TSDECL;
  TS(0);

  if (__builtin_expect(wv < 3, 1)) {
    if (mem < 3) { __builtin_amdgcn_s_barrier(); TSOUT(); return; }
    const int sa = grp * 13 + 15 - mem;
    const bool isQ = sa < 8;
    const int colblk = xcd + 8 * (isQ ? sa : sa < 17 ? sa - 8 : sa - 17);
    const int rs = isQ ? 8192 : 9216;
    const rsrc_t rw = mk(isQ ? p.wq : sa < 17 ? p.wg : p.wu, DM * rs);
    const rsrc_t rx = mk(p.x, NB * DM * 2);
    const int vo = ((n16 >> 3) * 32 + G * 8) * rs + (n16 & 7) * 16 + colblk * 128;
    int so = wv * 192 * rs;
    const int sstep = mem < 9 ? TV * 9 / (4 * 89) : TV * 9 / (4 * 81);
    unsigned snext = t0 + 2 * sstep;
    v4u r[3][8], xa[3];
#pragma unroll
    for (int i = 0; i < 24; ++i) {
      if (TV && i >= 6 && i % 3 == 0) { while ((int)(snext - NOW()) > 0) {} snext += sstep; SBAR(); }
      if (i >= WS) { if (i - WS < XPOS + 1) VMWAIT(WS + 2); else VMWAIT(WS - 1); }
      r[i >> 3][i & 7] = ld16<AUX_NT>(rw, vo, so);
      so += (i & 7) == 7 ? 57 * rs : rs;
      if (i == XPOS) {
#pragma unroll
        for (int u = 0; u < 3; ++u) xa[u] = ld16<0>(rx, (n16 & 7) * (DM * 2) + ((n16 >> 3) * 32 + G * 8 + u * 64) * 2, wv * 192 * 2);
      }
      SBAR();
    }
    __builtin_amdgcn_s_barrier();
    SBAR();
    TS(1);
    f32x4 acc[8];
#pragma unroll
    for (int j = 0; j < 8; ++j) acc[j] = f32x4{0.f, 0.f, 0.f, 0.f};
#pragma unroll
    for (int t = 0; t < 3; ++t) { tile_mma<0, 8>(r[t], xa[t], acc); SBAR(); }
    TS(4);
    if ((G >> 1) == (n16 >> 3)) {
      LAS unsigned char* q = L + L_RA + (wv * 2 + (n16 >> 3)) * SLOT + ((G & 1) * 4 * 32 + (n16 & 7) * 4) * 4;
#pragma unroll
      for (int rr = 0; rr < 4; ++rr) {
        *(LAS f32x4*)(q + rr * 128) = f32x4{acc[0][rr], acc[1][rr], acc[2][rr], acc[3][rr]};
        *(LAS f32x4*)(q + rr * 128 + 288 * 4) = f32x4{acc[4][rr], acc[5][rr], acc[6][rr], acc[7][rr]};
      }
    }
    TS(2);
    if (lds_arrive(L, L_CNTA, lane) == 2) {
      const rsrc_t rws = mk((char*)p.ws + WS_BASE, WS_END);
      lds_wait_ge(L, L_FT, 1);
      const unsigned tag = lds_ld(L, L_TAG);
      const int i8 = lane & 7, rb = isQ ? lane >> 3 : i8, q0 = isQ ? 2 * i8 : lane >> 3, q1 = isQ ? q0 + 1 : q0 + 8;
      const LAS unsigned char* p0 = L + L_RA + ((q0 & 1) * 288 + rb * 32 + (q0 >> 1) * 4) * 4;
      const LAS unsigned char* p1 = L + L_RA + ((q1 & 1) * 288 + rb * 32 + (q1 >> 1) * 4) * 4;
      f32x4 s0 = {0.f, 0.f, 0.f, 0.f}, s1 = {0.f, 0.f, 0.f, 0.f};
#pragma unroll
      for (int sl = 0; sl < 6; ++sl) { s0 += *(const LAS f32x4*)(p0 + sl * SLOT); s1 += *(const LAS f32x4*)(p1 + sl * SLOT); }
      const unsigned c0 = cvtpk(s0[0], s0[1]), c1 = cvtpk(s0[2], s0[3]), c2 = cvtpk(s1[0], s1[1]), c3 = cvtpk(s1[2], s1[3]);
      if (isQ) {
        const int o = WS_QG + (colblk * 8 + (lane >> 3)) * PSQ + i8 * 16;
        st16<AUX_SC1>(v4u{c0, tag, c1, tag}, rws, o);
        st16<AUX_SC1>(v4u{c2, tag, c3, tag}, rws, o + 128);
      } else {
        const int gs = colblk + (sa < 17 ? 0 : 72);
        st16<AUX_SC1>(v4u{c0, c1, c2, c3}, rws, WS_GU + (gs * 4 + G) * PSG + n16 * 16);
        asm volatile("s_waitcnt vmcnt(0)" ::: "memory");
        if (lane == 0) st4<AUX_SC1>(tag, rws, 0, WS_GF + gs * FS);
      }
      TS(3);
    }
    TSOUT();
    return;
  }

  const int kvh = 2 * xcd + grp;
  const rsrc_t rws = mk((char*)p.ws + WS_BASE, WS_END);
  const bool isO = mem < 9;
  const int du = (mem - 9) * 16 + ((kvh >> 1) | ((kvh & 1) << 3));
  const int dq = (du * 57) >> 9;
  const bool isD = !isO && du < 108;
  const int cc = isO ? mem : du - 9 * dq, chunk = isO ? kvh : 16 + dq;

  if (wv == 3) {
    const unsigned e0 = __builtin_amdgcn_raw_buffer_load_b32(rws, 0, WS_E, AUX_SC1);
    const v4u cp = ld16<0>(mk((const void*)(pc0 & ~127ull), CODE_BYTES), lane * 16, ii * 1024);
    if (lane < 5) *(LAS unsigned*)(L + 4 + lane * 4) = 0u;
    LGKM0();
    SBAR();
    __builtin_amdgcn_s_barrier();
    SBAR();
    const unsigned tag = __builtin_amdgcn_readfirstlane(e0) + 1u;
    if (lane == 0) *(LAS unsigned*)(L + L_TAG) = tag;
    lds_arrive(L, L_FT, lane);
    asm volatile("" ::"v"(cp));
    TS(1);
    if (ii < 9) {
      const int rb = (xcd >> 1) * 2 + (lane >> 5), l32 = lane & 31;
      const rsrc_t rx = mk(p.x, NB * DM * 2), rg = mk(p.gw, DM * 2);
      float gs = 0.f;
      v4u xv[3], gv[3];
#pragma unroll
      for (int u = 0; u < 3; ++u) {
        xv[u] = ld16<0>(rx, (u == 2 && l32 >= 8 ? NB : rb) * (DM * 2) + l32 * 16 + u * 512, 0);
        gv[u] = ld16<0>(rg, l32 * 16 + u * 512, 0);
      }
#pragma unroll
      for (int u = 0; u < 3; ++u)
#pragma unroll
        for (int d = 0; d < 4; ++d) gs = dot2(xv[u][d], gv[u][d], gs);
      gs += dpp<0xB1>(gs); gs += dpp<0x4E>(gs); gs += dpp<0x141>(gs); gs += dpp<0x128>(gs);
      gs = swap16_sum(gs, gs);
      if (l32 == 0) *(LAS float*)(L + L_GATE + (lane >> 5) * 4) = sigm(gs);
    }
    if (isO) {
      const unsigned z = __builtin_amdgcn_raw_buffer_load_b32(rws, lane < 16 || ii >= 9 || lane >= 44 ? WS_LS + (kvh * 16 + n16) * FS : WS_PF + (ii * 28 + lane - 16) * FS, 0, AUX_SC1);
      asm volatile("" ::"v"(z));
    }
    while ((int)(NOW() - t0) < QT0) __builtin_amdgcn_s_sleep(4);
    {
      const int c16 = 4 * G + (n16 & 3), h = (n16 >> 2) & 1;
      const int vo = ((kvh * 4 + 2 * h + (c16 >> 3)) * 8 + (mem >> 1)) * PSQ + (c16 & 7) * 16;
      v4u g0, g1;
#pragma nounroll
      for (unsigned n = 0; n < SPIN_G; ++n) {
        MEMFENCE();
        g0 = ld16<AUX_SC1>(rws, vo, WS_QG);
        g1 = ld16<AUX_SC1>(rws, vo + 128, WS_QG);
        const bool ok = g0[1] == tag && g0[3] == tag && g1[1] == tag && g1[3] == tag;
        if (__all(ok)) break;
        __builtin_amdgcn_s_sleep(2);
      }
      *(LAS v4u*)(L + L_QF + lane * 16) = v4u{g0[0], g0[2], g1[0], g1[2]};
      lds_arrive(L, L_FQ, lane);
      TS(2);
    }
    if (isD) {
      while ((int)((unsigned)__builtin_amdgcn_s_memrealtime() - t0) < GT0) __builtin_amdgcn_s_sleep(4);
      const int t = dq * 6 + min(n16, 5) + (G & 1) * 72;
#pragma nounroll
      for (unsigned n = 0; n < SPIN_G; ++n) {
        MEMFENCE();
        const unsigned f = __builtin_amdgcn_raw_buffer_load_b32(rws, t * FS, WS_GF, AUX_SC1);
        if (__all(f == tag)) break;
        __builtin_amdgcn_s_sleep(8);
      }
      MEMFENCE();
      TS(3);
      const bool up = n16 >= 8;
      const int gvo = WS_GU + (dq * 24 + G) * PSG + n16 * 16;
      v4u gg[6], uu[6];
#pragma unroll
      for (int u = 0; u < 6; ++u) { gg[u] = ld16<AUX_SC1>(rws, gvo, u * 4 * PSG); uu[u] = ld16<AUX_SC1>(rws, gvo, (72 + u) * 4 * PSG); }
#pragma unroll
      for (int u = 0; u < 12; ++u) {
        const unsigned h0 = hpair(gg[u >> 1][(u & 1) * 2], uu[u >> 1][(u & 1) * 2]), h1 = hpair(gg[u >> 1][(u & 1) * 2 + 1], uu[u >> 1][(u & 1) * 2 + 1]);
        *(LAS v4u*)(L + L_HF + u * 1024 + lane * 16) = v4u{up ? 0u : h0, up ? 0u : h1, up ? h0 : 0u, up ? h1 : 0u};
      }
      LGKM0();
      TS(4);
    }
    TSOUT();
    return;
  }

  const int w = wv - 4, b = mem >> 1, sp = mem & 1;
  const size_t kvoff = ((size_t)(b * HKV + kvh) * SK + (SK - WIN) + sp * 512) * (DH * 2) + w * 1024;
  const rsrc_t rk = mk((const char*)p.kc + kvoff, 131072 - w * 1024), rv = mk((const char*)p.vc + kvoff, 131072 - w * 1024);
  const int kvo = (n16 >> 2) * 256 + (G * 4 + (n16 & 3)) * 16, vvo = lane * 16;
  const char* cb = isO ? (const char*)p.wo + (size_t)(kvh * 256 + w * 64) * (DM * 2) : (const char*)p.wd + (size_t)(dq * 384 + w * 96) * (DM * 2);
  const rsrc_t rc = mk(cb + cc * 128, isO ? 64 * DM * 2 - cc * 128 : isD ? 96 * DM * 2 - cc * 128 : 0);
  const int cvo = G * (isO ? 16 : 8) * (DM * 2) + n16 * 8;
  const int pstep = mem < 3 ? TV * 16 / (PBASE + 71) : isO ? TV * 16 / 89 : TV * 16 / 81;
  int pnext = (mem < 3 ? PBASE : 18) * pstep;
  bool calm = false;
#define PACE(i) do { if (TV && ((i) % CHK) == 0) { const unsigned tt_ = t0 + (pnext >> 4); pnext += CHK * pstep; \
    int d_; do d_ = (int)(tt_ - NOW()); while (d_ > 0); calm = d_ > -LAG; SBAR(); } } while (0)
  if (PRIO) __builtin_amdgcn_s_setprio(PRIO);
  __builtin_amdgcn_s_barrier();
  SBAR();
  TS(1);
  v4u K[32], V[4][8];
#define THROTTLE(xh, xl) do { VMWAIT(WW - 1 + (xh)); if (WLO < WW && calm) VMWAIT(WLO - 1 + (xl)); } while (0)
#define ISSUE_K(j) do { PACE(j); if ((j) >= WLO) THROTTLE(0, 0); K[j] = ld16<AUX_NT>(rk, kvo, (j) * 4096); SBAR(); } while (0)
#define ISSUE_VN(j, xh, xl) do { SBAR(); PACE(j); THROTTLE(xh, xl); V[(j) >> 3][(j) & 7] = ld16<AUX_NT>(rv, vvo, (j) * 4096); SBAR(); } while (0)
#define ISSUE_V(j) ISSUE_VN(j, 0, 0)
#pragma unroll
  for (int j = 0; j < 32; ++j) ISSUE_K(j);
#pragma unroll
  for (int j = 0; j < 4; ++j) ISSUE_V(j);
  TS(2);
  lds_wait_ge(L, L_FQ, 1);
  TS(3);
  const v4u qf = *(LAS v4u*)(L + L_QF + lane * 16);
  const unsigned tag = lds_ld(L, L_TAG);
  float s[32];
#pragma unroll
  for (int j = 0; j < 32; ++j) {
    if ((j & 3) == 0) ISSUE_V(4 + (j >> 2));
    const f32x4 d = mfma(K[j], qf, f32x4{0.f, 0.f, 0.f, 0.f});
    s[j] = dpp<0xFF>(d[3]) + (dpp<0xAA>(d[2]) + (dpp<0x55>(d[1]) + d[0]));
  }
  ISSUE_V(12);
  float m = s[0];
#pragma unroll
  for (int j = 1; j < 32; ++j) m = fmaxf(m, s[j]);
  m = xrow_max(m);
  constexpr float C2 = 0.08838834764831845f * 1.4426950408889634f;
  const float mc = m * C2;
  float l = 0.f;
  v4u pf[4];
#pragma unroll
  for (int j = 0; j < 32; j += 2) {
    if ((j & 7) == 0) ISSUE_V(13 + (j >> 3));
    const float p0 = __builtin_amdgcn_exp2f(fmaf(s[j], C2, -mc)), p1 = __builtin_amdgcn_exp2f(fmaf(s[j + 1], C2, -mc));
    l += p0 + p1;
    pf[j >> 3][(j & 7) >> 1] = cvtpk(p0, p1);
  }
  ISSUE_V(17);
  l = xrow_sum(l);
  if (lane == 0 || lane == 4) *(LAS f32x2*)(L + L_ML + (w * 2 + (lane >> 2)) * 8) = f32x2{mc, l};
  LGKM0();
  if (lane == 0) __hip_atomic_fetch_add((LAS unsigned*)(L + L_CNTML), 1u, __ATOMIC_RELAXED, __HIP_MEMORY_SCOPE_WORKGROUP);
  TS(4);
  f32x4 acc[8];
#pragma unroll
  for (int j = 0; j < 8; ++j) acc[j] = f32x4{0.f, 0.f, 0.f, 0.f};
  ISSUE_V(18); tile_mma<0, 4>(V[0], pf[0], acc);
  ISSUE_V(19); tile_mma<4, 8>(V[0], pf[0], acc);

  float fw[4], lse;
#define MERGE_FACTORS() do { lds_wait_ge(L, L_CNTML, 4); f32x2 ml[4]; \
    _Pragma("unroll") for (int u = 0; u < 4; ++u) ml[u] = *(LAS f32x2*)(L + L_ML + (u * 2 + ((w == 2 ? lane : w) & 1)) * 8); \
    const float M = fmaxf(fmaxf(ml[0][0], ml[1][0]), fmaxf(ml[2][0], ml[3][0])); float Ls = 0.f; \
    _Pragma("unroll") for (int u = 0; u < 4; ++u) { fw[u] = __builtin_amdgcn_exp2f(ml[u][0] - M); Ls = fmaf(ml[u][1], fw[u], Ls); } \
    const float inv = __builtin_amdgcn_rcpf(Ls); \
    _Pragma("unroll") for (int u = 0; u < 4; ++u) fw[u] *= inv; \
    lse = M + __builtin_amdgcn_logf(Ls); } while (0)
#define STORE_LSE() do { SBAR(); st8<AUX_SC1>(v2u{__float_as_uint(lse), tag}, rws, w == 2 && lane < 2 ? lane * 8 : WS_END, WS_LS + (kvh * 16 + mem) * FS); SBAR(); } while (0)
#define ISSUE_C(e, KSR) c[e] = ld8<AUX_NT>(rc, cvo + ((e) & 3) * (DM * 2), (((e) >> 3) * (KSR) + ((e) & 4)) * (DM * 2))
#define CFRAG(dst, ks, j) _Pragma("unroll") for (int mm = 0; mm < 4; ++mm) \
    dst[mm] = ((j) & 1) ? phi(c[(ks) * 8 + 2 * mm + 1][(j) >> 1], c[(ks) * 8 + 2 * mm][(j) >> 1]) : plo(c[(ks) * 8 + 2 * mm + 1][(j) >> 1], c[(ks) * 8 + 2 * mm][(j) >> 1])
#define PERM_V3(mm) _Pragma("unroll") for (int j = 0; j < 8; ++j) bl[j][mm] = (j & 1) ? phi(V[3][2 * (mm) + 1][j >> 1], V[3][2 * (mm)][j >> 1]) : plo(V[3][2 * (mm) + 1][j >> 1], V[3][2 * (mm)][j >> 1])
#define FINISH_PV() do { \
    _Pragma("unroll") for (int j = 0; j < 8; ++j) { \
      bl[j][3] = (j & 1) ? phi(V[3][7][j >> 1], V[3][6][j >> 1]) : plo(V[3][7][j >> 1], V[3][6][j >> 1]); \
      acc[j] = mfma(pf[3], bl[j], acc[j]); } \
    TS(6); \
    if (lane < 32) { LAS float* q = (LAS float*)(L + L_OR + w * ORW) + lane; \
      _Pragma("unroll") for (int j = 0; j < 8; ++j) q[j * 36] = acc[j][0]; } \
    LGKM0(); __builtin_amdgcn_s_barrier(); MEMFENCE(); \
    if (w < 2) { const LAS float* q = (const LAS float*)(L + L_OR) + (lane & 3) * 72 + w * 16 + (lane >> 2); \
      float o0 = 0.f, o1 = 0.f; \
      _Pragma("unroll") for (int u = 0; u < 4; ++u) { o0 = fmaf(q[u * 288], fw[u], o0); o1 = fmaf(q[u * 288 + 36], fw[u], o1); } \
      st8<AUX_SC1>(v2u{cvtpk(o0, o1), tag}, rws, (lane >> 5) * PSO + (lane & 31) * 8, WS_OG + ((kvh * 16 + mem) * 4 + w * 2) * PSO); } \
    TS(7); } while (0)

  v4u bl[8];
  f32x4 ca[4];
  f32x2 wt[4];
  if (isO) {
    v2u c[16];
    v4u bf[8];
#define MINI(x, y) ((x) < (y) ? (x) : (y))
#define MAXI(x, y) ((x) > (y) ? (x) : (y))
#define CPAIR(sl) do { SBAR(); PACE(20 + (sl)); THROTTLE(MINI(sl, WW - 1), MINI(sl, WLO - 1)); ISSUE_C(2 * (sl), 8); ISSUE_C(2 * (sl) + 1, 8); SBAR(); } while (0)
#define ISSUE_VC(j) ISSUE_VN(j, MAXI(WW - 1 - ((j) - 20), 0), MAXI(WLO - 1 - ((j) - 20), 0))
    CPAIR(0); CPAIR(1);
    MERGE_FACTORS();
    CPAIR(2); CPAIR(3);
    CPAIR(4); tile_mma<0, 4>(V[1], pf[1], acc);
    CPAIR(5); tile_mma<4, 8>(V[1], pf[1], acc);
    CPAIR(6); CPAIR(7);
    ISSUE_VC(20); ISSUE_VC(21); ISSUE_VC(22); ISSUE_VC(23); ISSUE_VC(24); ISSUE_VC(25); ISSUE_VC(26); ISSUE_VC(27);
    ISSUE_V(28);
#pragma unroll
    for (int j = 0; j < 4; ++j) { CFRAG(bf[j], 0, j); asm volatile("" : "+v"(bf[j])); }
    ISSUE_V(29);
#pragma unroll
    for (int j = 0; j < 4; ++j) { CFRAG(bf[4 + j], 1, j); asm volatile("" : "+v"(bf[4 + j])); }
    ISSUE_V(30); tile_mma<0, 4>(V[2], pf[2], acc);
    ISSUE_V(31); tile_mma<4, 8>(V[2], pf[2], acc);
    TS(5);
    SBAR(); PERM_V3(0); SBAR(); PERM_V3(1);
    if (LSEPOS == 1) STORE_LSE();
    SBAR(); PERM_V3(2);
    if (LSEPOS == 2) STORE_LSE();
    SBAR();
    if (LSEPOS == 3) { VMWAIT(0); STORE_LSE(); }
    FINISH_PV();
    const int lvo = WS_LS + (kvh * 16 + ((n16 + 4 * w) & 15)) * FS;
    v4u lg = {0u, tag + 1u, 0u, 0u};
#pragma nounroll
    for (unsigned n = 0; n < SPIN_G; ++n) {
      MEMFENCE();
      if (lane < 16 && !(lg[1] == tag && lg[3] == tag)) lg = ld16<AUX_SC1>(rws, lvo, 0);
      if (__all(lane >= 16 || (lg[1] == tag && lg[3] == tag))) break;
    }
    TS(11);
    const int ovo = WS_OG + (kvh * 64 + ((n16 >> 2) & 1) * 32 + (n16 >> 3) * 4 + w) * PSO + G * 64 + (n16 & 3) * 16;
    v4u og[4];
#pragma unroll
    for (int u = 0; u < 4; ++u) og[u] = ld16<AUX_SC1>(rws, ovo, u * 8 * PSO);
    SBAR();
#pragma unroll
    for (int h = 0; h < 2; ++h) {
      const float own = __uint_as_float(lg[2 * h]);
      const float wo_ = __builtin_amdgcn_rcpf(1.f + __builtin_amdgcn_exp2f(dpp<0xB1>(own) - own));
#pragma unroll
      for (int s2 = 0; s2 < 2; ++s2)
        wt[h * 2 + s2] = f32x2{__builtin_bit_cast(float, __builtin_amdgcn_readlane(__builtin_bit_cast(int, wo_), s2)),
                               __builtin_bit_cast(float, __builtin_amdgcn_readlane(__builtin_bit_cast(int, wo_), 2 + s2))};
    }
    SBAR();
#pragma nounroll
    for (unsigned n = 0; n < SPIN_G; ++n) {
      unsigned bad = 0;
#pragma unroll
      for (int u = 0; u < 4; ++u) bad |= (og[u][1] ^ tag) | (og[u][3] ^ tag);
      if (__all(bad == 0)) break;
      MEMFENCE();
      if (bad != 0) {
#pragma unroll
        for (int u = 0; u < 4; ++u) og[u] = ld16<AUX_SC1>(rws, ovo, u * 8 * PSO);
      }
    }
    TS(8);
    st4<AUX_SC1>(tag, rws, w == 3 && lane == 0 ? 0 : WS_END, WS_PF + (cc * 28 + chunk) * FS);
    v4u a0, a1;
    const bool b0 = lane & 1, b1 = lane & 2;
#pragma unroll
    for (int d = 0; d < 2; ++d) {
      const unsigned r0 = og[0][2 * d], r1 = og[1][2 * d], r2 = og[2][2 * d], r3 = og[3][2 * d];
      const unsigned s0 = dppu<0xB1>(r0), s1 = dppu<0xB1>(r1), s2 = dppu<0xB1>(r2), s3 = dppu<0xB1>(r3);
      const unsigned x0 = b0 ? s1 : r0, x1 = b0 ? r1 : s0, x2 = b0 ? s3 : r2, x3 = b0 ? r3 : s2;
      const unsigned y0 = dppu<0x4E>(x0), y1 = dppu<0x4E>(x1), y2 = dppu<0x4E>(x2), y3 = dppu<0x4E>(x3);
      a0[d] = b1 ? y2 : x0; a1[d] = b1 ? x2 : y0;
      a0[2 + d] = b1 ? y3 : x1; a1[2 + d] = b1 ? x3 : y1;
    }
#pragma unroll
    for (int j = 0; j < 4; ++j) ca[j] = mfma(a0, bf[j], f32x4{0.f, 0.f, 0.f, 0.f});
#pragma unroll
    for (int j = 0; j < 4; ++j) ca[j] = mfma(a1, bf[4 + j], ca[j]);
  } else {
    v2u c[24];
    ISSUE_V(20); ISSUE_V(21);
    MERGE_FACTORS();
    ISSUE_V(22); ISSUE_V(23);
    ISSUE_V(24); tile_mma<0, 4>(V[1], pf[1], acc);
    ISSUE_V(25); tile_mma<4, 8>(V[1], pf[1], acc);
    ISSUE_V(26); ISSUE_V(27); ISSUE_V(28); ISSUE_V(29);
    ISSUE_V(30); tile_mma<0, 4>(V[2], pf[2], acc);
    ISSUE_V(31); tile_mma<4, 8>(V[2], pf[2], acc);
    TS(5);
#define CN(left) MINI((5 - (left)) * CSTEP, 24)
#define CSTEP_AT(left) do { SBAR(); VMWAIT((left) + CN(left)); _Pragma("unroll") for (int e_ = CN(left); e_ < CN((left) - 1); ++e_) ISSUE_C(e_, 32); SBAR(); } while (0)
    CSTEP_AT(5); PERM_V3(0);
    CSTEP_AT(4); PERM_V3(1);
    CSTEP_AT(3);
    CSTEP_AT(2); PERM_V3(2);
    CSTEP_AT(1);
    STORE_LSE();
    FINISH_PV();
    if (!isD) { TSOUT(); return; }
#pragma unroll
    for (int e = CN(0); e < 24; ++e) ISSUE_C(e, 32);
    SBAR();
    v4u af[3];
#pragma unroll
    for (int t = 0; t < 3; ++t) af[t] = *(const LAS v4u*)(L + L_HF + (w * 3 + t) * 1024 + lane * 16);
#pragma unroll
    for (int u = 0; u < 4; ++u) wt[u] = f32x2{1.f, 1.f};
#pragma unroll
    for (int j = 0; j < 4; ++j) ca[j] = f32x4{0.f, 0.f, 0.f, 0.f};
#pragma unroll
    for (int t = 0; t < 3; ++t)
#pragma unroll
      for (int j = 0; j < 4; ++j) { v4u f; CFRAG(f, t, j); ca[j] = mfma(af[t], f, ca[j]); }
    st4<AUX_SC1>(tag, rws, w == 3 && lane == 0 ? 0 : WS_END, WS_PF + (cc * 28 + chunk) * FS);
    TS(8);
  }
#pragma unroll
  for (int j = 0; j < 4; ++j) *(LAS f32x4*)(L + L_RC + w * RCW + j * RCJ + lane * 16) = ca[j];
  LGKM0();
  __builtin_amdgcn_s_barrier();
  MEMFENCE();
  {
    const LAS unsigned char* q = L + L_RC + (lane & 3) * RCJ + (w >> 1) * 256 + (lane >> 2) * 16 + (w & 1) * 8;
    f32x2 t = {0.f, 0.f};
#pragma unroll
    for (int u = 0; u < 4; ++u)
#pragma unroll
      for (int s2 = 0; s2 < 2; ++s2) t += *(const LAS f32x2*)(q + u * RCW + s2 * 512) * wt[(u >> 1) * 2 + s2];
    st8<AUX_SC1>(v2u{tagf(t[0], tag), tagf(t[1], tag)}, rws, (lane >> 5) * PSP + (lane & 31) * 8, WS_PS + ((cc * 4 + w) * 56 + chunk * 2) * PSP);
  }
  TS(9);
  if (ii < 9 && w == 0) {
    const f32x2 gt = *(const LAS f32x2*)(L + L_GATE);
    const f32x4 g4 = {gt[0], gt[1], gt[0], gt[1]}, h4 = {1.f - gt[0], 1.f - gt[1], 1.f - gt[0], 1.f - gt[1]};
    const int pvo = WS_PS + ((ii * 4 + (xcd >> 1)) * 56 + G * 2 + (xcd & 1)) * PSP + n16 * 16;
    unsigned f = tag + 1;
#pragma nounroll
    for (unsigned n = 0; n < SPIN_G; ++n) {
      MEMFENCE();
      if (f != tag && lane < 28) f = __builtin_amdgcn_raw_buffer_load_b32(rws, lane * FS, WS_PF + ii * 28 * FS, AUX_SC1);
      if (__all(f == tag || lane >= 28)) break;
    }
    v4u pg[7];
#pragma nounroll
    for (unsigned n = 0; n < SPIN_G; ++n) {
      MEMFENCE();
#pragma unroll
      for (int i = 0; i < 7; ++i) pg[i] = ld16<AUX_SC1>(rws, pvo, i * 8 * PSP);
      unsigned bad = 0;
#pragma unroll
      for (int i = 0; i < 7; ++i)
#pragma unroll
        for (int d = 0; d < 4; ++d) { pg[i][d] ^= tag & 0xFFu; bad |= pg[i][d]; }
      if (__all((bad & 0xFFu) == 0)) break;
    }
    const f32x4 a4 = (__builtin_bit_cast(f32x4, pg[0]) + __builtin_bit_cast(f32x4, pg[1])) + (__builtin_bit_cast(f32x4, pg[2]) + __builtin_bit_cast(f32x4, pg[3]));
    const f32x4 b4 = (__builtin_bit_cast(f32x4, pg[4]) + __builtin_bit_cast(f32x4, pg[5])) + __builtin_bit_cast(f32x4, pg[6]);
    const f32x4 r4 = g4 * a4 + h4 * b4;
    const float res = swap16_sum(swap32_sum(r4[0], r4[1]), swap32_sum(r4[2], r4[3]));
    const unsigned ob = cvtpk(res, res);
    ((GAS unsigned short*)p.out)[((xcd >> 1) * 2 + (lane >> 5)) * DM + ii * 64 + (xcd & 1) * 32 + n16 * 2 + (G & 1)] = (unsigned short)ob;
    if (wg == 0 && lane == 0) st4<AUX_SC1>(tag, rws, 0, WS_E);
    TS(10);
  }
  TSOUT();
}
}

extern "C" void kernel_launch(void* const* d_in, const int* in_sizes, int n_in, void* d_out, int out_size, void* d_ws, size_t ws_size,
                              hipStream_t stream) {
  Params p;
  p.x = d_in[0]; p.kc = d_in[1]; p.vc = d_in[2]; p.wq = d_in[3]; p.wo = d_in[4];
  p.wg = d_in[6]; p.wu = d_in[7]; p.wd = d_in[8]; p.gw = d_in[9];
  p.out = d_out; p.ws = d_ws;
  fused_kernel<<<256, 512, 0, stream>>>(p);
}
```

```cpp
#include <hip/hip_runtime.h>
#include <hip/hip_bf16.h>
#include <stdint.h>

#define STAMP 0
#define WW 8
#define WLO 6
#define LAG 40
#define WS 11
#define TAU 225
#define TOFF 14
#define CHK 4
#define CTAU 200
#define COFF 83
#ifndef NVQ
#define NVQ 8
#endif
#define XPOS 5
#define FVPOS 30
#define QT0 600
#define QJ 80
#define GT0 800
#define LSELEFT 2
#define SYNC 1
#define OAUX 0
#define NPRE 2
#define LGPOS 1
#define ST0 400
#define SAPP 24
#define CODE_BYTES 18048

namespace {
constexpr int NB = 8, DM = 576, HKV = 16, SK = 4096, DH = 128, WIN = 1024;
typedef unsigned v4u __attribute__((ext_vector_type(4)));
typedef unsigned v2u __attribute__((ext_vector_type(2)));
typedef float f32x4 __attribute__((ext_vector_type(4)));
typedef float f32x2 __attribute__((ext_vector_type(2)));
typedef short bf16x8 __attribute__((ext_vector_type(8)));
typedef __bf16 bf16x2 __attribute__((ext_vector_type(2)));
#define LAS __attribute__((address_space(3)))
#define GAS __attribute__((address_space(1)))
typedef __amdgpu_buffer_rsrc_t rsrc_t;
constexpr int NT = 2, SC1 = 16, RS3 = 0x00020000;

constexpr int WS_E = 0;
constexpr int WS_T0 = 1024;
constexpr int WS_GF = 4096;
constexpr int WS_LS = WS_GF + 144 * 128;
constexpr int WS_QG = WS_LS + 256 * 128;
constexpr int WS_GU = WS_QG + 64 * 2048;
constexpr int WS_OG = WS_GU + 144 * 1024;
constexpr int WS_PS = WS_OG + 256 * 1024;
constexpr int WS_END = WS_PS + 36 * 28 * 512;
constexpr int WS_TS = 8 << 20;

constexpr int L_TAG = 0, L_CNTA = 4, L_CNTML = 8, L_FQ = 12, L_FG = 16, L_FT = 20, L_FV = 24, L_DT = 28, L_GATE = 32;
constexpr int L_ML = 64;
constexpr int L_QF = 256;
constexpr int ORW = 8 * 36 * 4;
constexpr int L_OR = L_QF + 1024;
constexpr int SLOT = 576 * 4;
constexpr int L_RA = L_OR + 4 * ORW;
constexpr int RCJ = 1024 + 32, RCW = 4 * RCJ;
constexpr int L_RC = L_RA + 6 * SLOT;
constexpr int L_END = L_RC + 4 * RCW;

struct Params {
  const void* x; const void* kc; const void* vc; const void* wq; const void* wo; const void* wg; const void* wu; const void* wd; const void* gw;
  void* out; void* ws;
};

__device__ __forceinline__ rsrc_t mk(const void* p, unsigned bytes) { return __builtin_amdgcn_make_buffer_rsrc((void*)p, 0, (int)bytes, RS3); }
template <int AUX> __device__ __forceinline__ v4u ld16(rsrc_t r, int vo, int so) { return __builtin_amdgcn_raw_buffer_load_b128(r, vo, so, AUX); }
template <int AUX> __device__ __forceinline__ v2u ld8(rsrc_t r, int vo, int so) { return __builtin_amdgcn_raw_buffer_load_b64(r, vo, so, AUX); }
template <int AUX> __device__ __forceinline__ unsigned ld4(rsrc_t r, int vo, int so) { return __builtin_amdgcn_raw_buffer_load_b32(r, vo, so, AUX); }
template <int AUX> __device__ __forceinline__ void st16(v4u v, rsrc_t r, int vo) { __builtin_amdgcn_raw_buffer_store_b128(v, r, vo, 0, AUX); }
template <int AUX> __device__ __forceinline__ void st8(v2u v, rsrc_t r, int vo) { __builtin_amdgcn_raw_buffer_store_b64(v, r, vo, 0, AUX); }
template <int AUX> __device__ __forceinline__ void st4(unsigned v, rsrc_t r, int vo) { __builtin_amdgcn_raw_buffer_store_b32(v, r, vo, 0, AUX); }
__device__ __forceinline__ f32x4 mfma(v4u a, v4u b, f32x4 c) {
  return __builtin_amdgcn_mfma_f32_16x16x32_bf16(__builtin_bit_cast(bf16x8, a), __builtin_bit_cast(bf16x8, b), c, 0, 0, 0);
}
__device__ __forceinline__ unsigned cvtpk(float lo, float hi) {
  f32x2 v = {lo, hi};
  return __builtin_bit_cast(unsigned, __builtin_convertvector(v, bf16x2));
}
__device__ __forceinline__ unsigned tagf(float v, unsigned tag) { return __builtin_amdgcn_perm(__float_as_uint(v), tag, 0x07060500u); }
__device__ __forceinline__ float dot2(unsigned a, unsigned b, float c) {
  return __builtin_amdgcn_fdot2_f32_bf16(__builtin_bit_cast(bf16x2, a), __builtin_bit_cast(bf16x2, b), c, false);
}
__device__ __forceinline__ float bflo(unsigned u) { return __builtin_bit_cast(float, u << 16); }
__device__ __forceinline__ float bfhi(unsigned u) { return __builtin_bit_cast(float, u & 0xffff0000u); }
template <int CTRL> __device__ __forceinline__ float dpp(float x) {
  return __builtin_bit_cast(float, __builtin_amdgcn_mov_dpp(__builtin_bit_cast(int, x), CTRL, 0xf, 0xf, true));
}
template <int CTRL> __device__ __forceinline__ unsigned dppu(unsigned x) { return (unsigned)__builtin_amdgcn_mov_dpp((int)x, CTRL, 0xf, 0xf, true); }
__device__ __forceinline__ float rdlane(float x, int l) { return __builtin_bit_cast(float, __builtin_amdgcn_readlane(__builtin_bit_cast(int, x), l)); }
__device__ __forceinline__ float xrow_max(float x) {
  auto s = __builtin_amdgcn_permlane16_swap(__float_as_uint(x), __float_as_uint(x), false, false);
  x = fmaxf(__uint_as_float(s[0]), __uint_as_float(s[1]));
  auto t = __builtin_amdgcn_permlane32_swap(__float_as_uint(x), __float_as_uint(x), false, false);
  return fmaxf(__uint_as_float(t[0]), __uint_as_float(t[1]));
}
__device__ __forceinline__ float swap32_sum(float a, float b) {
  auto t = __builtin_amdgcn_permlane32_swap(__float_as_uint(a), __float_as_uint(b), false, false);
  return __uint_as_float(t[0]) + __uint_as_float(t[1]);
}
__device__ __forceinline__ float swap16_sum(float a, float b) {
  auto t = __builtin_amdgcn_permlane16_swap(__float_as_uint(a), __float_as_uint(b), false, false);
  return __uint_as_float(t[0]) + __uint_as_float(t[1]);
}
__device__ __forceinline__ float xrow_sum(float x) { x = swap16_sum(x, x); return swap32_sum(x, x); }
__device__ __forceinline__ float sigm(float x) { return __builtin_amdgcn_rcpf(1.f + __builtin_amdgcn_exp2f(-1.4426950408889634f * x)); }
__device__ __forceinline__ bool none(bool b) { return __builtin_amdgcn_ballot_w64(b) == 0ull; }
#define SBAR() __builtin_amdgcn_sched_barrier(0)
#define MEMFENCE() asm volatile("" ::: "memory")
#define LGKM0() asm volatile("s_waitcnt lgkmcnt(0)" ::: "memory")
#define PIN(x) asm volatile("" : "+v"(x))
constexpr int vmc(int n) { return (n & 15) | (7 << 4) | (15 << 8) | ((n >> 4) << 14); }
#define VMWAIT(n) __builtin_amdgcn_s_waitcnt(vmc(n))
#define NOW() ((unsigned)__builtin_amdgcn_s_memrealtime())

__device__ __forceinline__ int until(unsigned t) {
  int d;
  asm volatile("1:\n\ts_memrealtime vcc\n\ts_waitcnt lgkmcnt(0)\n\ts_sub_i32 %0, %1, vcc_lo\n\ts_cmp_gt_i32 %0, 0\n\ts_cbranch_scc1 1b" : "=&s"(d) : "s"(t) : "vcc", "scc");
  return d;
}
template <int N> __device__ __forceinline__ void vmwait_if_calm(int d) {
  asm volatile("s_cmp_gt_i32 %0, %1\n\ts_cbranch_scc0 1f\n\ts_waitcnt vmcnt(%2)\n1:" ::"s"(d), "i"(-LAG), "i"(N) : "scc");
}
template <int OFF, int SL> __device__ __forceinline__ void lds_wait_ge(int want) {
  int v, s, n;
  asm volatile("s_mov_b32 %2, 0x400000\n"
               "1:\n\tv_mov_b32 %0, 0\n\tds_read_b32 %0, %0 offset:%4\n\ts_waitcnt lgkmcnt(0)\n\tv_readfirstlane_b32 %1, %0\n\ts_cmp_ge_u32 %1, %3\n\ts_cbranch_scc1 2f\n\t"
               "s_sleep %5\n\ts_sub_u32 %2, %2, 1\n\ts_cbranch_scc0 1b\n2:"
               : "=&v"(v), "=&s"(s), "=&s"(n) : "s"(want), "i"(OFF), "i"(SL) : "scc", "memory");
}
constexpr unsigned SPIN_G = 1u << 16;

__device__ __forceinline__ unsigned lds_ld(LAS unsigned char* L, int off) { return __hip_atomic_load((LAS unsigned*)(L + off), __ATOMIC_RELAXED, __HIP_MEMORY_SCOPE_WORKGROUP); }
__device__ __forceinline__ unsigned lds_arrive(LAS unsigned char* L, int off, int lane) {
  LGKM0();
  unsigned old = 0;
  if (lane == 0) old = __hip_atomic_fetch_add((LAS unsigned*)(L + off), 1u, __ATOMIC_RELAXED, __HIP_MEMORY_SCOPE_WORKGROUP);
  old = __builtin_amdgcn_readfirstlane(old);
  MEMFENCE();
  return old;
}
__device__ __forceinline__ unsigned plo(unsigned hi_row, unsigned lo_row) { return __builtin_amdgcn_perm(hi_row, lo_row, 0x05040100u); }
__device__ __forceinline__ unsigned phi(unsigned hi_row, unsigned lo_row) { return __builtin_amdgcn_perm(hi_row, lo_row, 0x07060302u); }
template <int J0, int J1> __device__ __forceinline__ void tile_mma(const v4u (&r)[8], v4u a, f32x4 (&acc)[8]) {
#pragma unroll
  for (int j = J0; j < J1; ++j) {
    v4u bfr;
#pragma unroll
    for (int m = 0; m < 4; ++m) bfr[m] = (j & 1) ? phi(r[2 * m + 1][j >> 1], r[2 * m][j >> 1]) : plo(r[2 * m + 1][j >> 1], r[2 * m][j >> 1]);
    acc[j] = mfma(a, bfr, acc[j]);
  }
#pragma unroll
  for (int j = J0; j < J1; ++j) PIN(acc[j]);
}
__device__ __forceinline__ unsigned hpair(unsigned gp, unsigned up) {
  const float g0 = bflo(gp), g1 = bfhi(gp);
  return cvtpk(g0 * sigm(g0) * bflo(up), g1 * sigm(g1) * bfhi(up));
}

#define TS(i)
#define TSDECL
#define TSOUT()

typedef unsigned long long u64;
typedef u64 u64x8 __attribute__((ext_vector_type(8)));
typedef u64 u64x2 __attribute__((ext_vector_type(2)));
#define PINS(x) asm volatile("" : "+s"(x))

__global__ void __launch_bounds__(512, 2) fused_kernel(Params unused) {
  __shared__ __attribute__((aligned(16))) unsigned char smem[L_END + (STAMP ? 512 : 0)];
  LAS unsigned char* L = (LAS unsigned char*)smem;
  u64x8 ka; u64x2 kb; u64 kws, tnow, pc0;
  asm volatile("s_load_dwordx16 %0, %5, 0x0\n\ts_load_dwordx4 %1, %5, 0x40\n\ts_load_dwordx2 %2, %5, 0x50\n\ts_memrealtime %3\n\ts_getpc_b64 %4"
               : "=&s"(ka), "=&s"(kb), "=&s"(kws), "=&s"(tnow), "=&s"(pc0) : "s"(__builtin_amdgcn_kernarg_segment_ptr()));
  SBAR();
  const int tid = threadIdx.x, lane = tid & 63, wv = __builtin_amdgcn_readfirstlane(tid >> 6);
  const int wg = blockIdx.x, xcd = wg & 7, ii = wg >> 3, grp = ii >> 4, mem = ii & 15;
  const int kvh = 2 * xcd + grp;
  const int G = lane >> 4, n16 = lane & 15;
  const bool isE = mem >= 13;
  const int du = (mem - (isE ? 13 : 9)) * 16 + kvh;
  const int dq = (du * 57) >> 9, dc = du - 9 * dq;
  const int sa = grp * 13 + mem;
  const bool isQ = !isE && sa < 8;
  const int colblk = xcd + 8 * (sa < 8 ? sa : sa < 17 ? sa - 8 : sa - 17);
  int rs = isE ? DM * 2 : isQ ? 8192 : 9216;
  int vo = (G * (isE ? 16 : 8) + (n16 >> 3) * (isE ? 4 : 32)) * rs + (n16 & 7) * 16;
  int j3 = isE ? 5 * rs : rs, j7 = isE ? 53 * rs : 57 * rs;
  int so = wv * 192 * rs + (isE ? dq * (576 * DM * 2) + dc * 128 : colblk * 128);
  int wsz = isE ? (du >= 45 ? 0 : 4608 * DM * 2) : DM * rs;
  int wsel = isE ? 3 : isQ ? 0 : sa < 17 ? 1 : 2;
  int xvo = (n16 & 7) * (DM * 2) + ((n16 >> 3) * 32 + G * 8) * 2 + wv * 384 + (isE ? NB * DM * 2 : 0);
  PINS(rs); PIN(vo); PINS(j3); PINS(j7); PINS(so); PINS(wsz); PINS(wsel); PIN(xvo);
  SBAR();
  asm volatile("s_waitcnt lgkmcnt(0)" : "+s"(ka), "+s"(kb), "+s"(kws), "+s"(tnow));
  struct { const void *x, *kc, *vc, *wq, *wo, *wg, *wu, *wd, *gw; void *out, *ws; } p = {(const void*)ka[0], (const void*)ka[1], (const void*)ka[2], (const void*)ka[3], (const void*)ka[4],
      (const void*)ka[5], (const void*)ka[6], (const void*)ka[7], (const void*)kb[0], (void*)kb[1], (void*)kws};
  const unsigned t0 = (unsigned)tnow;
  TSDECL;

  if (__builtin_expect(wv < 3, 1)) {
    const u64 w01 = wsel & 1 ? ka[5] : ka[3], w23 = wsel & 1 ? ka[7] : ka[6];
    const rsrc_t rw = mk((const void*)(wsel & 2 ? w23 : w01), wsz);
    const rsrc_t rx = mk(p.x, NB * DM * 2);
    v4u r[3][8], xa[3];
#pragma unroll
    for (int i = 0; i < 24; ++i) {
      if (TAU && i >= 6 && i % 3 == 0) until(t0 + TOFF + i * TAU * 3 / 64);
      if (i >= WS) { if (i - WS < XPOS + 1) VMWAIT(WS + 2); else VMWAIT(WS - 1); }
      SBAR();
      r[i >> 3][i & 7] = ld16<NT>(rw, vo, so);
      so += (i & 7) == 7 ? j7 : (i & 7) == 3 ? j3 : rs;
      if (i == XPOS) {
#pragma unroll
        for (int u = 0; u < 3; ++u) xa[u] = ld16<0>(rx, xvo + u * 128, 0);
      }
      SBAR();
    }
    __builtin_amdgcn_s_barrier();
    SBAR();
    TS(1);
    const rsrc_t rws = mk(p.ws, WS_END);
    f32x4 acc[8];
#pragma unroll
    for (int j = 0; j < 8; ++j) acc[j] = f32x4{0.f, 0.f, 0.f, 0.f};
    if (!isE) {
#pragma unroll
      for (int t = 0; t < 3; ++t) { tile_mma<0, 8>(r[t], xa[t], acc); SBAR(); }
    } else {
      if (du >= 45) { TSOUT(); return; }
      v4u bfr[3][8];
#pragma unroll
      for (int t = 0; t < 3; ++t)
#pragma unroll
        for (int j = 0; j < 8; ++j) {
#pragma unroll
          for (int m = 0; m < 4; ++m) bfr[t][j][m] = (j & 1) ? phi(r[t][2 * m + 1][j >> 1], r[t][2 * m][j >> 1]) : plo(r[t][2 * m + 1][j >> 1], r[t][2 * m][j >> 1]);
          PIN(bfr[t][j]);
        }
      lds_wait_ge<L_FG, 4>(1);
      lds_wait_ge<L_FV, 4>(1);
      const int gvo = WS_GU + (dq * 9 + wv * 3) * 1024 + lane * 16;
      v4u gg[3], uu[3];
#pragma unroll
      for (int t = 0; t < 3; ++t) { gg[t] = ld16<SC1>(rws, gvo + t * 1024, 0); uu[t] = ld16<SC1>(rws, gvo + t * 1024, 72 * 1024); }
      SBAR();
      __builtin_amdgcn_s_barrier();
      SBAR();
#pragma unroll
      for (int t = 0; t < 3; ++t) {
        v4u ha;
#pragma unroll
        for (int d = 0; d < 4; ++d) ha[d] = hpair(gg[t][d], uu[t][d]);
#pragma unroll
        for (int j = 0; j < 8; ++j) acc[j] = mfma(ha, bfr[t][j], acc[j]);
      }
    }
    TS(4);
    if ((G >> 1) == (n16 >> 3)) {
      LAS unsigned char* q = L + L_RA + (wv * 2 + (n16 >> 3)) * SLOT + ((G & 1) * 4 * 32 + (n16 & 7) * 4) * 4;
#pragma unroll
      for (int rr = 0; rr < 4; ++rr) {
        *(LAS f32x4*)(q + rr * 128) = f32x4{acc[0][rr], acc[1][rr], acc[2][rr], acc[3][rr]};
        *(LAS f32x4*)(q + rr * 128 + 288 * 4) = f32x4{acc[4][rr], acc[5][rr], acc[6][rr], acc[7][rr]};
      }
    }
    TS(2);
    if (lds_arrive(L, L_CNTA, lane) == 2) {
      lds_wait_ge<L_FT, 1>(1);
      const unsigned tag = lds_ld(L, L_TAG);
      if (isE) {
#pragma unroll
        for (int i = 0; i < 2; ++i) {
          const int c2 = lane & 31, bp = 2 * i + (lane >> 5);
          const LAS unsigned char* q = L + L_RA + (((c2 >> 1) & 1) * 288 + bp * 64 + (c2 >> 2) * 4 + (c2 & 1) * 2) * 4;
          f32x2 s0 = {0.f, 0.f}, s1 = {0.f, 0.f};
#pragma unroll
          for (int sl = 0; sl < 6; ++sl) { s0 += *(const LAS f32x2*)(q + sl * SLOT); s1 += *(const LAS f32x2*)(q + sl * SLOT + 128); }
          st16<SC1>(v4u{tagf(s0[0], tag), tagf(s1[0], tag), tagf(s0[1], tag), tagf(s1[1], tag)}, rws, WS_PS + (dc * 4 + bp) * (28 * 512) + (16 + dq) * 512 + c2 * 16);
        }
      } else {
        const int i8 = lane & 7, rb = isQ ? lane >> 3 : i8, q0 = isQ ? 2 * i8 : 4 * G + (n16 >> 3), q1 = isQ ? q0 + 1 : q0 + 2;
        const LAS unsigned char* p0 = L + L_RA + ((q0 & 1) * 288 + rb * 32 + (q0 >> 1) * 4) * 4;
        const LAS unsigned char* p1 = L + L_RA + ((q1 & 1) * 288 + rb * 32 + (q1 >> 1) * 4) * 4;
        f32x4 s0 = {0.f, 0.f, 0.f, 0.f}, s1 = {0.f, 0.f, 0.f, 0.f};
#pragma unroll
        for (int sl = 0; sl < 6; ++sl) { s0 += *(const LAS f32x4*)(p0 + sl * SLOT); s1 += *(const LAS f32x4*)(p1 + sl * SLOT); }
        const unsigned c0 = cvtpk(s0[0], s0[1]), c1 = cvtpk(s0[2], s0[3]), c2 = cvtpk(s1[0], s1[1]), c3 = cvtpk(s1[2], s1[3]);
        if (isQ) {
          const int o = WS_QG + colblk * 2048 + (lane >> 3) * 256 + i8 * 16;
          st16<SC1 | NT>(v4u{c0, tag, c1, tag}, rws, o);
          st16<SC1 | NT>(v4u{c2, tag, c3, tag}, rws, o + 128);
        } else {
          const int gs = colblk + (sa < 17 ? 0 : 72);
          st16<SC1>(v4u{c0, c1, c2, c3}, rws, WS_GU + gs * 1024 + lane * 16);
          asm volatile("s_waitcnt vmcnt(0)" ::: "memory");
          if (lane < 8) st16<SC1>(v4u{tag, tag, tag, tag}, rws, WS_GF + gs * 128 + lane * 16);
        }
      }
      TS(3);
    }
    TSOUT();
    return;
  }

  const rsrc_t rws = mk(p.ws, WS_END);
  const bool isO = mem < 9, isD = !isO && !isE && du < 63;
  const bool isR = isE && du < 36;

  if (wv == 3) {
    const unsigned e0 = ld4<SC1>(rws, 0, WS_E);
    if (SYNC && ii == 0 && lane == 0) st4<SC1>(t0, rws, WS_T0 + xcd * 4);
    const v4u cp = ld16<0>(mk((const void*)(pc0 & ~127ull), CODE_BYTES), lane * 16, ii * 1024);
    if (lane < 7) *(LAS unsigned*)(L + 4 + lane * 4) = 0u;
    LGKM0();
    SBAR();
    __builtin_amdgcn_s_barrier();
    SBAR();
    const unsigned tag = __builtin_amdgcn_readfirstlane(e0) + 1u;
    if (lane == 0) *(LAS unsigned*)(L + L_TAG) = tag;
    lds_arrive(L, L_FT, lane);
    asm volatile("" ::"v"(cp));
    TS(1);
    if (isR) {
      const int rb = (du & 3) * 2 + (lane >> 5), l32 = lane & 31;
      const rsrc_t rx = mk(p.x, NB * DM * 2), rg = mk(p.gw, DM * 2);
      float gs = 0.f;
      v4u xv[3], gv[3];
#pragma unroll
      for (int u = 0; u < 3; ++u) {
        xv[u] = ld16<0>(rx, (u == 2 && l32 >= 8 ? NB : rb) * (DM * 2) + l32 * 16 + u * 512, 0);
        gv[u] = ld16<0>(rg, l32 * 16 + u * 512, 0);
      }
#pragma unroll
      for (int u = 0; u < 3; ++u)
#pragma unroll
        for (int d = 0; d < 4; ++d) gs = dot2(xv[u][d], gv[u][d], gs);
      gs += dpp<0xB1>(gs); gs += dpp<0x4E>(gs); gs += dpp<0x141>(gs); gs += dpp<0x128>(gs);
      gs = swap16_sum(gs, gs);
      if (l32 == 0) *(LAS float*)(L + L_GATE + (lane >> 5) * 4) = sigm(gs);
    }
    if (isO) {
      const unsigned z = ld4<SC1>(rws, lane < 16 ? WS_LS + (kvh * 16 + lane) * 128 : WS_END, 0);
      asm volatile("" ::"v"(z));
    }
    unsigned tl = t0;
    if (SYNC) {
      while ((int)(NOW() - t0) < ST0) __builtin_amdgcn_s_sleep(2);
      int d = (int)(ld4<SC1>(rws, (lane & 7) * 4, WS_T0) - t0);
      d = (unsigned)(d + 256) < 512u ? d : 0;
      int sm = d, mx = d;
      sm += (int)dppu<0xB1>(sm); sm += (int)dppu<0x4E>(sm); sm += (int)dppu<0x141>(sm);
      mx = max(mx, (int)dppu<0xB1>(mx)); mx = max(mx, (int)dppu<0x4E>(mx)); mx = max(mx, (int)dppu<0x141>(mx));
      if (lane == 0) *(volatile LAS int*)(L + L_DT) = sm >> 3;
      tl = t0 + __builtin_amdgcn_readfirstlane(mx);
    }
    while ((int)(NOW() - tl) < QT0 + (((wg >> 4) * 5 + xcd * 2) & 15) * QJ / 16) __builtin_amdgcn_s_sleep(2);
    {
      const int c16 = 4 * G + (n16 & 3), h = (n16 >> 2) & 1;
      const int vo = (kvh * 4 + 2 * h + (c16 >> 3)) * 2048 + (mem >> 1) * 256 + (c16 & 7) * 16;
      v4u g0, g1;
#pragma nounroll
      for (unsigned n = 0; n < SPIN_G; ++n) {
        MEMFENCE();
        g0 = ld16<SC1 | NT>(rws, vo, WS_QG);
        g1 = ld16<SC1 | NT>(rws, vo + 128, WS_QG);
        if (none(((g0[1] ^ tag) | (g0[3] ^ tag) | (g1[1] ^ tag) | (g1[3] ^ tag)) != 0)) break;
        __builtin_amdgcn_s_sleep(4);
      }
      *(LAS v4u*)(L + L_QF + lane * 16) = v4u{g0[0], g0[2], g1[0], g1[2]};
      lds_arrive(L, L_FQ, lane);
      TS(2);
    }
    if (!isO) {
      while ((int)(NOW() - tl) < GT0) __builtin_amdgcn_s_sleep(4);
      const int t = min(isE ? dq * 9 + min(n16, 8) : 45 + dq * 4 + (n16 & 3), 71) + (G & 1) * 72;
#pragma nounroll
      for (unsigned n = 0; n < SPIN_G; ++n) {
        MEMFENCE();
        if (none(ld4<SC1>(rws, t * 128, WS_GF) != tag)) break;
        __builtin_amdgcn_s_sleep(8);
      }
      MEMFENCE();
      TS(3);
    }
    lds_arrive(L, L_FG, lane);
    TSOUT();
    return;
  }

  const int w = wv - 4;
  const int cc = isO ? mem : dc, chunk = isO ? kvh : 21 + dq;
  const size_t kvoff = ((size_t)((mem >> 1) * HKV + kvh) * SK + (SK - WIN) + (mem & 1) * 512) * (DH * 2) + w * 1024;
  const rsrc_t rk = mk((const char*)p.kc + kvoff, 131072 - w * 1024), rv = mk((const char*)p.vc + kvoff, 131072 - w * 1024);
  const int kvo = (n16 >> 2) * 256 + (G * 4 + (n16 & 3)) * 16, vvo = lane * 16;
  const char* cb = isO ? (const char*)p.wo + (size_t)(kvh * 256 + w * 64) * (DM * 2) : (const char*)p.wd + (size_t)(2880 + dq * 256 + w * 64) * (DM * 2);
  const rsrc_t rc = mk(cb + cc * 128, isO || (isD && dq * 4 + w < 27) ? 63 * DM * 2 + 128 : 0);
  int cvo = G * 16 * (DM * 2) + n16 * 8;
  int a_lse = w == 2 && lane < 2 ? WS_LS + (kvh * 16 + mem) * 128 + lane * 8 : WS_END;
  int a_gu = isD ? WS_GU + min(45 + dq * 4 + w, 71) * 1024 + lane * 16 : WS_END;
  int l_ow = L_OR + w * ORW + (lane & 31) * 4;
  int l_or = L_OR + ((lane & 3) * 72 + (w & 1) * 16 + (lane >> 2)) * 4;
  int a_opub = w < 2 ? WS_OG + (kvh * 16 + mem) * 1024 + w * 512 + lane * 8 : WS_END;
  int a_1 = isO ? WS_LS + (kvh * 16 + ((n16 + 4 * w) & 15)) * 128 : WS_PS + du * (28 * 512) + (lane >> 3) * 512 + w * 128 + (lane & 7) * 16;
  int a_2 = isO ? WS_OG + kvh * 16384 + ((n16 >> 2) & 1) * 8192 + (n16 >> 3) * 1024 + w * 256 + G * 64 + (n16 & 3) * 16
                : (lane & 8 ? NB * DM : ((du & 3) * 2 + (lane >> 5)) * DM + (du >> 2) * 64 + w * 16 + (lane & 7) * 2 + ((lane >> 4) & 1)) * 2;
  int a_lg = isO && lane < 16 ? a_1 : WS_END;
  int l_cw = L_RC + w * RCW + lane * 8;
  int l_cr = L_RC + (lane & 3) * RCJ + (w & 1) * 512 + (w >> 1) * 128 + (lane >> 2) * 8;
  int a_ppub = WS_PS + (cc * 4 + w) * (28 * 512) + chunk * 512 + lane * 8;
  PIN(cvo); PIN(a_lse); PIN(a_gu); PIN(l_ow); PIN(l_or); PIN(a_opub); PIN(a_1); PIN(a_2); PIN(l_cw); PIN(l_cr); PIN(a_ppub); PIN(a_lg);
  __builtin_amdgcn_s_setprio(2);
  __builtin_amdgcn_s_barrier();
  SBAR();
  TS(1);
  v4u K[32], V[4][8];
  int lag = 0;
  unsigned tb = t0;
#define SLOT_GO(i) do { SBAR(); if (TAU && ((i) % CHK) == 0) { if (SYNC && (i) == SAPP) tb = t0 + __builtin_amdgcn_readfirstlane(lds_ld(L, L_DT)); lag = until(tb + TOFF + ((18 + (i)) * TAU) / 16); } \
    if ((i) >= WW) VMWAIT(WW - 1); if (WLO < WW && (i) >= WLO) vmwait_if_calm<WLO - 1>(lag); SBAR(); } while (0)
#define ISSUE_K(j) do { SLOT_GO(j); K[j] = ld16<NT>(rk, kvo, (j) * 4096); SBAR(); } while (0)
#define ISSUE_V(j) do { SLOT_GO(32 + (j)); V[(j) >> 3][(j) & 7] = ld16<NT>(rv, vvo, (j) * 4096); if ((j) == FVPOS) *(volatile LAS unsigned*)(L + L_FV) = 1u; SBAR(); } while (0)
#pragma unroll
  for (int j = 0; j < 32; ++j) ISSUE_K(j);
#pragma unroll
  for (int j = 0; j < NVQ; ++j) ISSUE_V(j);
  TS(2);
  lds_wait_ge<L_FQ, 1>(1);
  TS(3);
  const v4u qf = *(LAS v4u*)(L + L_QF + lane * 16);
  const unsigned tag = lds_ld(L, L_TAG);
  float s[32];
#pragma unroll
  for (int j = 0; j < 32; j += 4) {
    ISSUE_V(NVQ + (j >> 2));
    f32x4 d[4];
#pragma unroll
    for (int k = 0; k < 4; ++k) d[k] = mfma(K[j + k], qf, f32x4{0.f, 0.f, 0.f, 0.f});
    SBAR();
#pragma unroll
    for (int k = 0; k < 4; ++k) { s[j + k] = dpp<0x55>(d[k][1]) + d[k][0]; PIN(s[j + k]); }
#pragma unroll
    for (int k = 0; k < 4; ++k) { s[j + k] = dpp<0xAA>(d[k][2]) + s[j + k]; PIN(s[j + k]); }
#pragma unroll
    for (int k = 0; k < 4; ++k) { s[j + k] = dpp<0xFF>(d[k][3]) + s[j + k]; PIN(s[j + k]); }
  }
  ISSUE_V(NVQ + 8);
  float m = s[0];
#pragma unroll
  for (int j = 1; j < 32; ++j) m = fmaxf(m, s[j]);
  m = xrow_max(m);
  constexpr float C2 = 0.08838834764831845f * 1.4426950408889634f;
  const float mc = m * C2;
  float l = 0.f;
  v4u pf[4];
#pragma unroll
  for (int j = 0; j < 32; j += 2) {
    if ((j & 7) == 0) ISSUE_V(NVQ + 9 + (j >> 3));
    const float p0 = __builtin_amdgcn_exp2f(fmaf(s[j], C2, -mc)), p1 = __builtin_amdgcn_exp2f(fmaf(s[j + 1], C2, -mc));
    l += p0 + p1;
    pf[j >> 3][(j & 7) >> 1] = cvtpk(p0, p1);
    if ((j & 7) == 6) PIN(pf[j >> 3]);
  }
  ISSUE_V(NVQ + 13);
  l = xrow_sum(l);
  *(LAS f32x2*)(L + ((lane & 59) == 0 ? L_ML + (w * 2 + (lane >> 2)) * 8 : l_cw)) = f32x2{mc, l};
  LGKM0();
  asm volatile("ds_add_u32 %0, %1" ::"v"(lane == 0 ? L_CNTML : l_cw), "v"(1) : "memory");
  TS(4);
  f32x4 acc[8];
#pragma unroll
  for (int j = 0; j < 8; ++j) acc[j] = f32x4{0.f, 0.f, 0.f, 0.f};
  ISSUE_V(NVQ + 14); tile_mma<0, 4>(V[0], pf[0], acc);
  ISSUE_V(NVQ + 15); tile_mma<4, 8>(V[0], pf[0], acc);
#pragma unroll
  for (int j = NVQ + 16; j < 24; ++j) ISSUE_V(j);
  ISSUE_V(24); tile_mma<0, 4>(V[1], pf[1], acc);
  ISSUE_V(25); tile_mma<4, 8>(V[1], pf[1], acc);
  ISSUE_V(26);
  lds_wait_ge<L_CNTML, 1>(4);
  float fw[4], lse;
  {
    f32x2 ml[4];
#pragma unroll
    for (int u = 0; u < 4; ++u) ml[u] = *(LAS f32x2*)(L + L_ML + (u * 2 + ((w == 2 ? lane : w) & 1)) * 8);
    const float M = fmaxf(fmaxf(ml[0][0], ml[1][0]), fmaxf(ml[2][0], ml[3][0]));
    float Ls = 0.f;
#pragma unroll
    for (int u = 0; u < 4; ++u) { fw[u] = __builtin_amdgcn_exp2f(ml[u][0] - M); Ls = fmaf(ml[u][1], fw[u], Ls); }
    const float inv = __builtin_amdgcn_rcpf(Ls);
#pragma unroll
    for (int u = 0; u < 4; ++u) { fw[u] *= inv; PIN(fw[u]); }
    lse = M + __builtin_amdgcn_logf(Ls);
    PIN(lse);
  }
  ISSUE_V(27);
  lds_wait_ge<L_FG, 1>(1);
  ISSUE_V(28);
  ISSUE_V(29);
  ISSUE_V(30); tile_mma<0, 4>(V[2], pf[2], acc);
  ISSUE_V(31); tile_mma<4, 8>(V[2], pf[2], acc);
  TS(5);
  v2u c[16];
  v4u bl[8];
#define ISSUE_C(e) c[e] = ld8<NT>(rc, cvo + ((e) & 3) * (DM * 2), ((e) >> 2) * 4 * (DM * 2))
#define PERM_V3(mm) _Pragma("unroll") for (int j = 0; j < 8; ++j) bl[j][mm] = (j & 1) ? phi(V[3][2 * (mm) + 1][j >> 1], V[3][2 * (mm)][j >> 1]) : plo(V[3][2 * (mm) + 1][j >> 1], V[3][2 * (mm)][j >> 1]); _Pragma("unroll") for (int j = 0; j < 8; ++j) PIN(bl[j])
#define CPAIR(sl) do { SBAR(); if (CTAU) until(tb + TOFF + (COFF * TAU + (sl) * CTAU) / 16); SBAR(); ISSUE_C(2 * (sl)); ISSUE_C(2 * (sl) + 1); SBAR(); } while (0)
  SBAR();
  const v4u gg = ld16<SC1>(rws, a_gu, 0), uu = ld16<SC1>(rws, a_gu, 72 * 1024);
  if (NPRE == 8) { CPAIR(0); CPAIR(1); CPAIR(2); CPAIR(3); CPAIR(4); CPAIR(5); CPAIR(6); CPAIR(7); PERM_V3(0); SBAR(); PERM_V3(1); SBAR(); }
  else {
    if (NPRE > 0) CPAIR(0);
    PERM_V3(0); SBAR();
    if (NPRE > 1) CPAIR(1);
    PERM_V3(1); SBAR();
    if (NPRE > 2) CPAIR(2);
    if (NPRE > 3) CPAIR(3);
    if (NPRE > 4) CPAIR(4);
    if (NPRE > 5) CPAIR(5);
  }
  if (LSELEFT >= 3) { VMWAIT(2 + 2 * NPRE + LSELEFT); st8<SC1>(v2u{__float_as_uint(lse), tag}, rws, a_lse); SBAR(); }
  PERM_V3(2); SBAR();
  if (LSELEFT < 3) { VMWAIT(2 + 2 * NPRE + LSELEFT); st8<SC1>(v2u{__float_as_uint(lse), tag}, rws, a_lse); SBAR(); }
  v4u lg;
  if (LGPOS == 2) { lg = ld16<SC1>(rws, a_lg, 0); SBAR(); }
#pragma unroll
  for (int j = 0; j < 8; ++j) {
    bl[j][3] = (j & 1) ? phi(V[3][7][j >> 1], V[3][6][j >> 1]) : plo(V[3][7][j >> 1], V[3][6][j >> 1]);
    acc[j] = mfma(pf[3], bl[j], acc[j]);
  }
  SBAR();
  TS(6);
#pragma unroll
  for (int j = 0; j < 8; ++j) *(LAS float*)(L + l_ow + j * 144) = acc[j][0];
  LGKM0();
  __builtin_amdgcn_s_barrier();
  MEMFENCE();
  {
    f32x2 o = {0.f, 0.f};
#pragma unroll
    for (int u = 0; u < 4; ++u) o += f32x2{*(const LAS float*)(L + l_or + u * ORW), *(const LAS float*)(L + l_or + u * ORW + 144)} * fw[u];
    st8<SC1>(v2u{cvtpk(o[0], o[1]), tag}, rws, a_opub);
  }
  TS(7);
  SBAR();
  if (LGPOS == 1) { lg = ld16<SC1>(rws, a_lg, 0); SBAR(); }
#pragma unroll
  for (int sl = NPRE; sl < 8; ++sl) CPAIR(sl);
  if (LGPOS == 0) { lg = ld16<SC1>(rws, a_lg, 0); SBAR(); }

  if (isO || isD) {
    f32x2 wt[4];
    v4u a0, a1, bf[8];
#define CFRAGS() _Pragma("unroll") for (int j = 0; j < 8; ++j) { _Pragma("unroll") for (int mm = 0; mm < 4; ++mm) \
      bf[j][mm] = (j & 1) ? phi(c[(j >> 2) * 8 + 2 * mm + 1][(j & 3) >> 1], c[(j >> 2) * 8 + 2 * mm][(j & 3) >> 1]) : plo(c[(j >> 2) * 8 + 2 * mm + 1][(j & 3) >> 1], c[(j >> 2) * 8 + 2 * mm][(j & 3) >> 1]); \
      PIN(bf[j]); }
    if (isO) {
      bool lbad = ((lg[1] ^ tag) | (lg[3] ^ tag)) != 0 && lane < 16;
      if (!none(lbad)) {
#pragma nounroll
        for (unsigned n = 0; n < SPIN_G; ++n) {
          MEMFENCE();
          if (lbad) { lg = ld16<SC1>(rws, a_1, 0); lbad = ((lg[1] ^ tag) | (lg[3] ^ tag)) != 0; }
          if (none(lbad)) break;
        }
      }
      TS(11);
      v4u og[4];
#pragma unroll
      for (int u = 0; u < 4; ++u) og[u] = ld16<SC1>(rws, a_2 + (u & 1) * 2048, (u >> 1) * 4096);
      SBAR();
#pragma unroll
      for (int h = 0; h < 2; ++h) {
        const float own = __uint_as_float(lg[2 * h]);
        const float wo_ = __builtin_amdgcn_rcpf(1.f + __builtin_amdgcn_exp2f(dpp<0xB1>(own) - own));
#pragma unroll
        for (int s2 = 0; s2 < 2; ++s2) wt[h * 2 + s2] = f32x2{rdlane(wo_, s2), rdlane(wo_, 2 + s2)};
      }
      CFRAGS();
      SBAR();
#pragma nounroll
      for (unsigned n = 0; n < SPIN_G; ++n) {
        unsigned bad = 0;
#pragma unroll
        for (int u = 0; u < 4; ++u) bad |= (og[u][1] ^ tag) | (og[u][3] ^ tag);
        if (none(bad != 0)) break;
        MEMFENCE();
        if (bad != 0) {
#pragma unroll
          for (int u = 0; u < 4; ++u) og[u] = ld16<SC1>(rws, a_2 + (u & 1) * 2048, (u >> 1) * 4096);
        }
      }
      unsigned x0, x1, x2, x3, x4, x5, x6, x7, y0, y1, y2, y3, y4, y5, y6, y7;
#define CM1 " quad_perm:[1,0,3,2] row_mask:0xf bank_mask:0xf\n\t"
#define CM2 " quad_perm:[2,3,0,1] row_mask:0xf bank_mask:0xf\n\t"
      asm volatile("s_nop 1\n\ts_mov_b64 vcc, %24\n\t"
                   "v_cndmask_b32_dpp %0, %17, %16, vcc" CM1 "v_cndmask_b32_dpp %2, %19, %18, vcc" CM1 "v_cndmask_b32_dpp %4, %21, %20, vcc" CM1 "v_cndmask_b32_dpp %6, %23, %22, vcc" CM1
                   "s_not_b64 vcc, vcc\n\t"
                   "v_cndmask_b32_dpp %1, %16, %17, vcc" CM1 "v_cndmask_b32_dpp %3, %18, %19, vcc" CM1 "v_cndmask_b32_dpp %5, %20, %21, vcc" CM1 "v_cndmask_b32_dpp %7, %22, %23, vcc" CM1
                   "s_mov_b64 vcc, %25\n\t"
                   "v_cndmask_b32_dpp %8, %2, %0, vcc" CM2 "v_cndmask_b32_dpp %9, %6, %4, vcc" CM2 "v_cndmask_b32_dpp %10, %3, %1, vcc" CM2 "v_cndmask_b32_dpp %11, %7, %5, vcc" CM2
                   "s_not_b64 vcc, vcc\n\t"
                   "v_cndmask_b32_dpp %12, %0, %2, vcc" CM2 "v_cndmask_b32_dpp %13, %4, %6, vcc" CM2 "v_cndmask_b32_dpp %14, %1, %3, vcc" CM2 "v_cndmask_b32_dpp %15, %5, %7, vcc" CM2
                   "s_nop 1"
                   : "=&v"(x0), "=&v"(x1), "=&v"(x2), "=&v"(x3), "=&v"(x4), "=&v"(x5), "=&v"(x6), "=&v"(x7),
                     "=&v"(y0), "=&v"(y1), "=&v"(y2), "=&v"(y3), "=&v"(y4), "=&v"(y5), "=&v"(y6), "=&v"(y7)
                   : "v"(og[0][0]), "v"(og[1][0]), "v"(og[2][0]), "v"(og[3][0]), "v"(og[0][2]), "v"(og[1][2]), "v"(og[2][2]), "v"(og[3][2]),
                     "s"(0x5555555555555555ull), "s"(0x3333333333333333ull)
                   : "vcc", "scc");
      a0 = v4u{y0, y1, y2, y3}; a1 = v4u{y4, y5, y6, y7};
    } else {
      const bool up = n16 >= 8;
      const unsigned h0 = hpair(gg[0], uu[0]), h1 = hpair(gg[1], uu[1]), h2 = hpair(gg[2], uu[2]), h3 = hpair(gg[3], uu[3]);
      a0 = v4u{up ? 0u : h0, up ? 0u : h1, up ? h0 : 0u, up ? h1 : 0u};
      a1 = v4u{up ? 0u : h2, up ? 0u : h3, up ? h2 : 0u, up ? h3 : 0u};
      PIN(a0); PIN(a1);
#pragma unroll
      for (int u = 0; u < 4; ++u) wt[u] = f32x2{1.f, 1.f};
      SBAR();
      CFRAGS();
    }
    TS(8);
    f32x4 ca[4];
#pragma unroll
    for (int j = 0; j < 4; ++j) ca[j] = mfma(a0, bf[j], f32x4{0.f, 0.f, 0.f, 0.f});
#pragma unroll
    for (int j = 0; j < 4; ++j) ca[j] = mfma(a1, bf[4 + j], ca[j]);
    SBAR();
#pragma unroll
    for (int j = 0; j < 4; ++j) {
      *(LAS f32x2*)(L + l_cw + j * RCJ) = f32x2{ca[j][0], ca[j][1]};
      *(LAS f32x2*)(L + l_cw + j * RCJ + 512) = f32x2{ca[j][2], ca[j][3]};
    }
    LGKM0();
    __builtin_amdgcn_s_barrier();
    MEMFENCE();
    f32x2 rd[8];
#pragma unroll
    for (int u = 0; u < 8; ++u) rd[u] = *(const LAS f32x2*)(L + l_cr + (u >> 1) * RCW + (u & 1) * 256);
    SBAR();
    f32x2 t0_ = {0.f, 0.f}, t1_ = {0.f, 0.f};
#pragma unroll
    for (int u = 0; u < 4; ++u) { t0_ += rd[2 * u] * wt[(u >> 1) * 2]; t1_ += rd[2 * u + 1] * wt[(u >> 1) * 2 + 1]; }
    t0_ += t1_;
    st8<SC1>(v2u{tagf(t0_[0], tag), tagf(t0_[1], tag)}, rws, a_ppub);
    TS(9);
  } else if (isR) {
    const f32x2 gt = *(const LAS f32x2*)(L + L_GATE);
    f32x2 ht = {1.f - gt[0], 1.f - gt[1]};
    PIN(ht);
    const unsigned t8 = tag & 0xFFu, t8h = lane < 32 ? t8 : 0u;
    const int a_3 = lane < 32 ? a_1 : WS_END;
    const rsrc_t ro = mk(p.out, NB * DM * 2);
    v4u pg[4];
    unsigned bad = 1;
#pragma nounroll
    for (unsigned n = 0; n < SPIN_G; ++n) {
      MEMFENCE();
      if (bad) {
#pragma unroll
        for (int i = 0; i < 3; ++i) pg[i] = ld16<SC1>(rws, a_1, i * 4096);
        pg[3] = ld16<SC1>(rws, a_3, 3 * 4096);
        bad = 0;
#pragma unroll
        for (int i = 0; i < 4; ++i)
#pragma unroll
          for (int d = 0; d < 4; ++d) { pg[i][d] ^= i < 3 ? t8 : t8h; bad |= pg[i][d]; }
        bad &= 0xFFu;
      }
      if (none(bad != 0)) break;
      __builtin_amdgcn_s_sleep(1);
    }
    const f32x4 a4 = __builtin_bit_cast(f32x4, pg[0]) + __builtin_bit_cast(f32x4, pg[1]), b4 = __builtin_bit_cast(f32x4, pg[2]) + __builtin_bit_cast(f32x4, pg[3]);
    const f32x2 r0 = gt * f32x2{a4[0], a4[1]} + ht * f32x2{b4[0], b4[1]}, r1 = gt * f32x2{a4[2], a4[3]} + ht * f32x2{b4[2], b4[3]};
    float res = swap16_sum(swap32_sum(r0[0], r0[1]), swap32_sum(r1[0], r1[1]));
    res += dpp<0x128>(res);
    __builtin_amdgcn_raw_buffer_store_b16((unsigned short)cvtpk(res, res), ro, a_2, 0, OAUX);
    if (du == 0 && w == 0 && lane == 0) st4<SC1>(tag, rws, WS_E);
    TS(10);
  }
  TSOUT();
}
}

extern "C" void kernel_launch(void* const* d_in, const int* in_sizes, int n_in, void* d_out, int out_size, void* d_ws, size_t ws_size,
                              hipStream_t stream) {
  Params p;
  p.x = d_in[0]; p.kc = d_in[1]; p.vc = d_in[2]; p.wq = d_in[3]; p.wo = d_in[4];
  p.wg = d_in[6]; p.wu = d_in[7]; p.wd = d_in[8]; p.gw = d_in[9];
  p.out = d_out; p.ws = d_ws;
  fused_kernel<<<256, 512, 0, stream>>>(p);
}
```

```cpp
#include <hip/hip_runtime.h>
#include <hip/hip_bf16.h>
#include <stdint.h>

#define STAMP 0
#define WW 8
#define WLO 5
#define LAG 30
#define WS 11
#define TAU 229
#define TOFF 14
#define CHK 4
#define CTAU TAU
#define COFF 83
#ifndef NVQ
#define NVQ 8
#endif
#define XPOS 5
#define FVPOS 30
#define QT0 600
#define QJ 80
#define GT0 800
#define LSELEFT 2
#define SYNC 1
#define OAUX 0
#define NPRE 2
#define LGPOS 1
#define ST0 400
#define SAPP 24
#define LGSL 5
#define DIRECT 35
#define OPRE 1
#define CWIN 12
#define CLEAD 10
#define DCLK 190
#define RCLK 120
#define RPRE 1
#define CODE_BYTES 18304

namespace {
constexpr int NB = 8, DM = 576, HKV = 16, SK = 4096, DH = 128, WIN = 1024;
typedef unsigned v4u __attribute__((ext_vector_type(4)));
typedef unsigned v2u __attribute__((ext_vector_type(2)));
typedef float f32x4 __attribute__((ext_vector_type(4)));
typedef float f32x2 __attribute__((ext_vector_type(2)));
typedef short bf16x8 __attribute__((ext_vector_type(8)));
typedef __bf16 bf16x2 __attribute__((ext_vector_type(2)));
#define LAS __attribute__((address_space(3)))
#define GAS __attribute__((address_space(1)))
typedef __amdgpu_buffer_rsrc_t rsrc_t;
constexpr int NT = 2, SC1 = 16, RS3 = 0x00020000;

constexpr int WS_E = 0;
constexpr int WS_T0 = 1024;
constexpr int WS_GF = 4096;
constexpr int WS_LS = WS_GF + 144 * 128;
constexpr int WS_QG = WS_LS + 256 * 128;
constexpr int WS_GU = WS_QG + 64 * 2048;
constexpr int WS_OG = WS_GU + 144 * 1024;
constexpr int WS_PS = WS_OG + 256 * 1024;
constexpr int WS_END = WS_PS + 36 * 28 * 512;
constexpr int WS_TS = 8 << 20;

constexpr int L_TAG = 0, L_CNTA = 4, L_CNTML = 8, L_FQ = 12, L_FG = 16, L_FT = 20, L_FV = 24, L_DT = 28, L_CNTO = 32, L_GATE = 40;
constexpr int L_ML = 64;
constexpr int L_QF = 256;
constexpr int ORW = 8 * 36 * 4;
constexpr int L_OR = L_QF + 1024;
constexpr int SLOT = 576 * 4;
constexpr int L_RA = L_OR + 4 * ORW;
constexpr int RCJ = 1024 + 32, RCW = 4 * RCJ;
constexpr int L_RC = L_RA + 6 * SLOT;
constexpr int L_DUMP = L_RC + 4 * RCW;
constexpr int L_END = L_DUMP + 512;

struct Params {
  const void* x; const void* kc; const void* vc; const void* wq; const void* wo; const void* wg; const void* wu; const void* wd; const void* gw;
  void* out; void* ws;
};

__device__ __forceinline__ rsrc_t mk(const void* p, unsigned bytes) { return __builtin_amdgcn_make_buffer_rsrc((void*)p, 0, (int)bytes, RS3); }
template <int AUX> __device__ __forceinline__ v4u ld16(rsrc_t r, int vo, int so) { return __builtin_amdgcn_raw_buffer_load_b128(r, vo, so, AUX); }
template <int AUX> __device__ __forceinline__ v2u ld8(rsrc_t r, int vo, int so) { return __builtin_amdgcn_raw_buffer_load_b64(r, vo, so, AUX); }
template <int AUX> __device__ __forceinline__ unsigned ld4(rsrc_t r, int vo, int so) { return __builtin_amdgcn_raw_buffer_load_b32(r, vo, so, AUX); }
template <int AUX> __device__ __forceinline__ void st16(v4u v, rsrc_t r, int vo) { __builtin_amdgcn_raw_buffer_store_b128(v, r, vo, 0, AUX); }
template <int AUX> __device__ __forceinline__ void st8(v2u v, rsrc_t r, int vo) { __builtin_amdgcn_raw_buffer_store_b64(v, r, vo, 0, AUX); }
template <int AUX> __device__ __forceinline__ void st4(unsigned v, rsrc_t r, int vo) { __builtin_amdgcn_raw_buffer_store_b32(v, r, vo, 0, AUX); }
__device__ __forceinline__ f32x4 mfma(v4u a, v4u b, f32x4 c) {
  return __builtin_amdgcn_mfma_f32_16x16x32_bf16(__builtin_bit_cast(bf16x8, a), __builtin_bit_cast(bf16x8, b), c, 0, 0, 0);
}
__device__ __forceinline__ unsigned cvtpk(float lo, float hi) {
  f32x2 v = {lo, hi};
  return __builtin_bit_cast(unsigned, __builtin_convertvector(v, bf16x2));
}
__device__ __forceinline__ unsigned tagf(float v, unsigned tag) { return __builtin_amdgcn_perm(__float_as_uint(v), tag, 0x07060500u); }
__device__ __forceinline__ float dot2(unsigned a, unsigned b, float c) {
  return __builtin_amdgcn_fdot2_f32_bf16(__builtin_bit_cast(bf16x2, a), __builtin_bit_cast(bf16x2, b), c, false);
}
__device__ __forceinline__ float bflo(unsigned u) { return __builtin_bit_cast(float, u << 16); }
__device__ __forceinline__ float bfhi(unsigned u) { return __builtin_bit_cast(float, u & 0xffff0000u); }
template <int CTRL> __device__ __forceinline__ float dpp(float x) {
  return __builtin_bit_cast(float, __builtin_amdgcn_mov_dpp(__builtin_bit_cast(int, x), CTRL, 0xf, 0xf, true));
}
template <int CTRL> __device__ __forceinline__ unsigned dppu(unsigned x) { return (unsigned)__builtin_amdgcn_mov_dpp((int)x, CTRL, 0xf, 0xf, true); }
__device__ __forceinline__ float rdlane(float x, int l) { return __builtin_bit_cast(float, __builtin_amdgcn_readlane(__builtin_bit_cast(int, x), l)); }
__device__ __forceinline__ float xrow_max(float x) {
  auto s = __builtin_amdgcn_permlane16_swap(__float_as_uint(x), __float_as_uint(x), false, false);
  x = fmaxf(__uint_as_float(s[0]), __uint_as_float(s[1]));
  auto t = __builtin_amdgcn_permlane32_swap(__float_as_uint(x), __float_as_uint(x), false, false);
  return fmaxf(__uint_as_float(t[0]), __uint_as_float(t[1]));
}
__device__ __forceinline__ float swap32_sum(float a, float b) {
  auto t = __builtin_amdgcn_permlane32_swap(__float_as_uint(a), __float_as_uint(b), false, false);
  return __uint_as_float(t[0]) + __uint_as_float(t[1]);
}
__device__ __forceinline__ float swap16_sum(float a, float b) {
  auto t = __builtin_amdgcn_permlane16_swap(__float_as_uint(a), __float_as_uint(b), false, false);
  return __uint_as_float(t[0]) + __uint_as_float(t[1]);
}
__device__ __forceinline__ float xrow_sum(float x) { x = swap16_sum(x, x); return swap32_sum(x, x); }
__device__ __forceinline__ float sigm(float x) { return __builtin_amdgcn_rcpf(1.f + __builtin_amdgcn_exp2f(-1.4426950408889634f * x)); }
__device__ __forceinline__ bool none(bool b) { return __builtin_amdgcn_ballot_w64(b) == 0ull; }
#define SBAR() __builtin_amdgcn_sched_barrier(0)
#define MEMFENCE() asm volatile("" ::: "memory")
#define LGKM0() asm volatile("s_waitcnt lgkmcnt(0)" ::: "memory")
#define PIN(x) asm volatile("" : "+v"(x))
constexpr int vmc(int n) { return (n & 15) | (7 << 4) | (15 << 8) | ((n >> 4) << 14); }
#define VMWAIT(n) __builtin_amdgcn_s_waitcnt(vmc(n))
#define NOW() ((unsigned)__builtin_amdgcn_s_memrealtime())

__device__ __forceinline__ int until(unsigned t) {
  int d;
  asm volatile("1:\n\ts_memrealtime vcc\n\ts_waitcnt lgkmcnt(0)\n\ts_sub_i32 %0, %1, vcc_lo\n\ts_cmp_gt_i32 %0, 0\n\ts_cbranch_scc1 1b" : "=&s"(d) : "s"(t) : "vcc", "scc");
  return d;
}
template <int N> __device__ __forceinline__ void vmwait_if_calm(int d) {
  asm volatile("s_cmp_gt_i32 %0, %1\n\ts_cbranch_scc0 1f\n\ts_waitcnt vmcnt(%2)\n1:" ::"s"(d), "i"(-LAG), "i"(N) : "scc");
}
template <int OFF, int SL> __device__ __forceinline__ void lds_wait_ge(int want) {
  int v, s, n;
  asm volatile("s_mov_b32 %2, 0x400000\n"
               "1:\n\tv_mov_b32 %0, 0\n\tds_read_b32 %0, %0 offset:%4\n\ts_waitcnt lgkmcnt(0)\n\tv_readfirstlane_b32 %1, %0\n\ts_cmp_ge_u32 %1, %3\n\ts_cbranch_scc1 2f\n\t"
               "s_sleep %5\n\ts_sub_u32 %2, %2, 1\n\ts_cbranch_scc0 1b\n2:"
               : "=&v"(v), "=&s"(s), "=&s"(n) : "s"(want), "i"(OFF), "i"(SL) : "scc", "memory");
}
constexpr unsigned SPIN_G = 1u << 16;

__device__ __forceinline__ unsigned lds_ld(LAS unsigned char* L, int off) { return __hip_atomic_load((LAS unsigned*)(L + off), __ATOMIC_RELAXED, __HIP_MEMORY_SCOPE_WORKGROUP); }
__device__ __forceinline__ unsigned lds_arrive(LAS unsigned char* L, int off, int lane) {
  LGKM0();
  unsigned old = 0;
  if (lane == 0) old = __hip_atomic_fetch_add((LAS unsigned*)(L + off), 1u, __ATOMIC_RELAXED, __HIP_MEMORY_SCOPE_WORKGROUP);
  old = __builtin_amdgcn_readfirstlane(old);
  MEMFENCE();
  return old;
}
__device__ __forceinline__ unsigned plo(unsigned hi_row, unsigned lo_row) { return __builtin_amdgcn_perm(hi_row, lo_row, 0x05040100u); }
__device__ __forceinline__ unsigned phi(unsigned hi_row, unsigned lo_row) { return __builtin_amdgcn_perm(hi_row, lo_row, 0x07060302u); }
template <int J0, int J1> __device__ __forceinline__ void tile_mma(const v4u (&r)[8], v4u a, f32x4 (&acc)[8]) {
#pragma unroll
  for (int j = J0; j < J1; ++j) {
    v4u bfr;
#pragma unroll
    for (int m = 0; m < 4; ++m) bfr[m] = (j & 1) ? phi(r[2 * m + 1][j >> 1], r[2 * m][j >> 1]) : plo(r[2 * m + 1][j >> 1], r[2 * m][j >> 1]);
    acc[j] = mfma(a, bfr, acc[j]);
  }
#pragma unroll
  for (int j = J0; j < J1; ++j) PIN(acc[j]);
}
__device__ __forceinline__ unsigned hpair(unsigned gp, unsigned up) {
  const float g0 = bflo(gp), g1 = bfhi(gp);
  return cvtpk(g0 * sigm(g0) * bflo(up), g1 * sigm(g1) * bfhi(up));
}

#define TS(i)
#define TSDECL
#define TSOUT()

typedef unsigned long long u64;
typedef u64 u64x8 __attribute__((ext_vector_type(8)));
typedef u64 u64x2 __attribute__((ext_vector_type(2)));
#define PINS(x) asm volatile("" : "+s"(x))

__global__ void __launch_bounds__(512, 2) fused_kernel(Params unused) {
  __shared__ __attribute__((aligned(16))) unsigned char smem[L_END + (STAMP ? 512 : 0)];
  LAS unsigned char* L = (LAS unsigned char*)smem;
  u64x8 ka; u64x2 kb; u64 kws, tnow, pc0;
  asm volatile("s_load_dwordx16 %0, %5, 0x0\n\ts_load_dwordx4 %1, %5, 0x40\n\ts_load_dwordx2 %2, %5, 0x50\n\ts_memrealtime %3\n\ts_getpc_b64 %4"
               : "=&s"(ka), "=&s"(kb), "=&s"(kws), "=&s"(tnow), "=&s"(pc0) : "s"(__builtin_amdgcn_kernarg_segment_ptr()));
  SBAR();
  const int tid = threadIdx.x, lane = tid & 63, wv = __builtin_amdgcn_readfirstlane(tid >> 6);
  const int wg = blockIdx.x, xcd = wg & 7, ii = wg >> 3, grp = ii >> 4, mem = ii & 15;
  const int kvh = 2 * xcd + grp;
  const int G = lane >> 4, n16 = lane & 15;
  const bool isE = mem >= 13;
  const int du = (mem - (isE ? 13 : 9)) * 16 + kvh;
  const int dq = (du * 57) >> 9, dc = du - 9 * dq;
  const int sa = grp * 13 + mem;
  const bool isQ = !isE && sa < 8;
  const int colblk = xcd + 8 * (sa < 8 ? sa : sa < 17 ? sa - 8 : sa - 17);
  int rs = isE ? DM * 2 : isQ ? 8192 : 9216;
  int vo = (G * (isE ? 16 : 8) + (n16 >> 3) * (isE ? 4 : 32)) * rs + (n16 & 7) * 16;
  int j3 = isE ? 5 * rs : rs, j7 = isE ? 53 * rs : 57 * rs;
  int so = wv * 192 * rs + (isE ? dq * (576 * DM * 2) + dc * 128 : colblk * 128);
  int wsz = isE ? (du >= 45 ? 0 : 4608 * DM * 2) : DM * rs;
  int wsel = isE ? 3 : isQ ? 0 : sa < 17 ? 1 : 2;
  int xvo = (n16 & 7) * (DM * 2) + ((n16 >> 3) * 32 + G * 8) * 2 + wv * 384 + (isE ? NB * DM * 2 : 0);
  PINS(rs); PIN(vo); PINS(j3); PINS(j7); PINS(so); PINS(wsz); PINS(wsel); PIN(xvo);
  SBAR();
  asm volatile("s_waitcnt lgkmcnt(0)" : "+s"(ka), "+s"(kb), "+s"(kws), "+s"(tnow));
  struct { const void *x, *kc, *vc, *wq, *wo, *wg, *wu, *wd, *gw; void *out, *ws; } p = {(const void*)ka[0], (const void*)ka[1], (const void*)ka[2], (const void*)ka[3], (const void*)ka[4],
      (const void*)ka[5], (const void*)ka[6], (const void*)ka[7], (const void*)kb[0], (void*)kb[1], (void*)kws};
  const unsigned t0 = (unsigned)tnow;
  const bool isO = mem < 9, isD = !isO && !isE && du < 63, hasC = isO || isD;
  TSDECL;

  if (__builtin_expect(wv < 3, 1)) {
    const u64 w01 = wsel & 1 ? ka[5] : ka[3], w23 = wsel & 1 ? ka[7] : ka[6];
    const rsrc_t rw = mk((const void*)(wsel & 2 ? w23 : w01), wsz);
    const rsrc_t rx = mk(p.x, NB * DM * 2);
    v4u r[3][8], xa[3];
#pragma unroll
    for (int i = 0; i < 24; ++i) {
      if (TAU && i >= 6 && i % 3 == 0) until(t0 + TOFF + i * TAU * 3 / 64);
      if (i >= WS) { if (i - WS < XPOS + 1) VMWAIT(WS + 2); else VMWAIT(WS - 1); }
      SBAR();
      r[i >> 3][i & 7] = ld16<0>(rw, vo, so);
      so += (i & 7) == 7 ? j7 : (i & 7) == 3 ? j3 : rs;
      if (i == XPOS) {
#pragma unroll
        for (int u = 0; u < 3; ++u) xa[u] = ld16<0>(rx, xvo + u * 128, 0);
      }
      SBAR();
    }
    __builtin_amdgcn_s_barrier();
    SBAR();
    TS(1);
    const rsrc_t rws = mk(p.ws, WS_END);
    f32x4 acc[8];
#pragma unroll
    for (int j = 0; j < 8; ++j) acc[j] = f32x4{0.f, 0.f, 0.f, 0.f};
    if (!isE) {
#pragma unroll
      for (int t = 0; t < 3; ++t) { tile_mma<0, 8>(r[t], xa[t], acc); SBAR(); }
    } else {
      if (du >= 45) { TSOUT(); return; }
      v4u bfr[3][8];
#pragma unroll
      for (int t = 0; t < 3; ++t)
#pragma unroll
        for (int j = 0; j < 8; ++j) {
#pragma unroll
          for (int m = 0; m < 4; ++m) bfr[t][j][m] = (j & 1) ? phi(r[t][2 * m + 1][j >> 1], r[t][2 * m][j >> 1]) : plo(r[t][2 * m + 1][j >> 1], r[t][2 * m][j >> 1]);
          PIN(bfr[t][j]);
        }
      lds_wait_ge<L_FG, 4>(1);
      lds_wait_ge<L_FV, 4>(1);
      const int gvo = WS_GU + (dq * 9 + wv * 3) * 1024 + lane * 16;
      v4u gg[3], uu[3];
#pragma unroll
      for (int t = 0; t < 3; ++t) { gg[t] = ld16<SC1>(rws, gvo + t * 1024, 0); uu[t] = ld16<SC1>(rws, gvo + t * 1024, 72 * 1024); }
      SBAR();
#pragma unroll
      for (int t = 0; t < 3; ++t) {
        v4u ha;
#pragma unroll
        for (int d = 0; d < 4; ++d) ha[d] = hpair(gg[t][d], uu[t][d]);
#pragma unroll
        for (int j = 0; j < 8; ++j) acc[j] = mfma(ha, bfr[t][j], acc[j]);
      }
    }
    TS(4);
    if ((G >> 1) == (n16 >> 3)) {
      LAS unsigned char* q = L + L_RA + (wv * 2 + (n16 >> 3)) * SLOT + ((G & 1) * 4 * 32 + (n16 & 7) * 4) * 4;
#pragma unroll
      for (int rr = 0; rr < 4; ++rr) {
        *(LAS f32x4*)(q + rr * 128) = f32x4{acc[0][rr], acc[1][rr], acc[2][rr], acc[3][rr]};
        *(LAS f32x4*)(q + rr * 128 + 288 * 4) = f32x4{acc[4][rr], acc[5][rr], acc[6][rr], acc[7][rr]};
      }
    }
    TS(2);
    if (lds_arrive(L, L_CNTA, lane) == 2) {
      lds_wait_ge<L_FT, 1>(1);
      const unsigned tag = lds_ld(L, L_TAG);
      if (isE) {
#pragma unroll
        for (int i = 0; i < 2; ++i) {
          const int c2 = lane & 31, bp = 2 * i + (lane >> 5);
          const LAS unsigned char* q = L + L_RA + (((c2 >> 1) & 1) * 288 + bp * 64 + (c2 >> 2) * 4 + (c2 & 1) * 2) * 4;
          f32x2 s0 = {0.f, 0.f}, s1 = {0.f, 0.f};
#pragma unroll
          for (int sl = 0; sl < 6; ++sl) { s0 += *(const LAS f32x2*)(q + sl * SLOT); s1 += *(const LAS f32x2*)(q + sl * SLOT + 128); }
          st16<SC1>(v4u{tagf(s0[0], tag), tagf(s1[0], tag), tagf(s0[1], tag), tagf(s1[1], tag)}, rws, WS_PS + (dc * 4 + bp) * (28 * 512) + (16 + dq) * 512 + c2 * 16);
        }
      } else {
        const int i8 = lane & 7, rb = isQ ? lane >> 3 : i8, q0 = isQ ? 2 * i8 : 4 * G + (n16 >> 3), q1 = isQ ? q0 + 1 : q0 + 2;
        const LAS unsigned char* p0 = L + L_RA + ((q0 & 1) * 288 + rb * 32 + (q0 >> 1) * 4) * 4;
        const LAS unsigned char* p1 = L + L_RA + ((q1 & 1) * 288 + rb * 32 + (q1 >> 1) * 4) * 4;
        f32x4 s0 = {0.f, 0.f, 0.f, 0.f}, s1 = {0.f, 0.f, 0.f, 0.f};
#pragma unroll
        for (int sl = 0; sl < 6; ++sl) { s0 += *(const LAS f32x4*)(p0 + sl * SLOT); s1 += *(const LAS f32x4*)(p1 + sl * SLOT); }
        const unsigned c0 = cvtpk(s0[0], s0[1]), c1 = cvtpk(s0[2], s0[3]), c2 = cvtpk(s1[0], s1[1]), c3 = cvtpk(s1[2], s1[3]);
        if (isQ) {
          const int o = WS_QG + colblk * 2048 + (lane >> 3) * 256 + i8 * 16;
          st16<SC1 | NT>(v4u{c0, tag, c1, tag}, rws, o);
          st16<SC1 | NT>(v4u{c2, tag, c3, tag}, rws, o + 128);
        } else {
          const int gs = colblk + (sa < 17 ? 0 : 72);
          st16<SC1>(v4u{c0, c1, c2, c3}, rws, WS_GU + gs * 1024 + lane * 16);
          asm volatile("s_waitcnt vmcnt(0)" ::: "memory");
          if (lane < 8) st16<SC1>(v4u{tag, tag, tag, tag}, rws, WS_GF + gs * 128 + lane * 16);
        }
      }
      TS(3);
    }
    if (!hasC) { TSOUT(); return; }
  } else if (wv == 3) {
    const rsrc_t rws = mk(p.ws, WS_END);
    const bool isR = isE && du < 36;
    const unsigned e0 = ld4<SC1>(rws, 0, WS_E);
    if (SYNC && ii == 0 && lane == 0) st4<SC1>(t0, rws, WS_T0 + xcd * 4);
    const v4u cp = ld16<0>(mk((const void*)(pc0 & ~127ull), CODE_BYTES), lane * 16, ii * 1024);
    if (lane < 8) *(LAS unsigned*)(L + 4 + lane * 4) = 0u;
    LGKM0();
    SBAR();
    __builtin_amdgcn_s_barrier();
    SBAR();
    const unsigned tag = __builtin_amdgcn_readfirstlane(e0) + 1u;
    if (lane == 0) *(LAS unsigned*)(L + L_TAG) = tag;
    lds_arrive(L, L_FT, lane);
    asm volatile("" ::"v"(cp));
    TS(1);
    if (isR) {
      const int rb = (du & 3) * 2 + (lane >> 5), l32 = lane & 31;
      const rsrc_t rx = mk(p.x, NB * DM * 2), rg = mk(p.gw, DM * 2);
      float gs = 0.f;
      v4u xv[3], gv[3];
#pragma unroll
      for (int u = 0; u < 3; ++u) {
        xv[u] = ld16<0>(rx, (u == 2 && l32 >= 8 ? NB : rb) * (DM * 2) + l32 * 16 + u * 512, 0);
        gv[u] = ld16<0>(rg, l32 * 16 + u * 512, 0);
      }
#pragma unroll
      for (int u = 0; u < 3; ++u)
#pragma unroll
        for (int d = 0; d < 4; ++d) gs = dot2(xv[u][d], gv[u][d], gs);
      gs += dpp<0xB1>(gs); gs += dpp<0x4E>(gs); gs += dpp<0x141>(gs); gs += dpp<0x128>(gs);
      gs = swap16_sum(gs, gs);
      if (l32 == 0) *(LAS float*)(L + L_GATE + (lane >> 5) * 4) = sigm(gs);
    }
    if (isO) {
      const unsigned z = ld4<SC1>(rws, lane < 16 ? WS_LS + (kvh * 16 + lane) * 128 : OPRE && lane < 32 && mem < 8 ? WS_OG + kvh * 16384 + mem * 2048 + (lane - 16) * 128 : WS_END, 0);
      asm volatile("" ::"v"(z));
    }
    unsigned tl = t0;
    if (SYNC) {
      while ((int)(NOW() - t0) < ST0) __builtin_amdgcn_s_sleep(2);
      int d = (int)(ld4<SC1>(rws, (lane & 7) * 4, WS_T0) - t0);
      d = (unsigned)(d + 256) < 512u ? d : 0;
      int sm = d, mx = d;
      sm += (int)dppu<0xB1>(sm); sm += (int)dppu<0x4E>(sm); sm += (int)dppu<0x141>(sm);
      mx = max(mx, (int)dppu<0xB1>(mx)); mx = max(mx, (int)dppu<0x4E>(mx)); mx = max(mx, (int)dppu<0x141>(mx));
      if (lane == 0) *(volatile LAS int*)(L + L_DT) = sm >> 3;
      tl = t0 + __builtin_amdgcn_readfirstlane(mx);
    }
    while ((int)(NOW() - tl) < QT0 + (((wg >> 4) * 5 + xcd * 2) & 15) * QJ / 16) __builtin_amdgcn_s_sleep(2);
    {
      const int c16 = 4 * G + (n16 & 3), h = (n16 >> 2) & 1;
      const int vo = (kvh * 4 + 2 * h + (c16 >> 3)) * 2048 + (mem >> 1) * 256 + (c16 & 7) * 16;
      v4u g0, g1;
#pragma nounroll
      for (unsigned n = 0; n < SPIN_G; ++n) {
        MEMFENCE();
        g0 = ld16<SC1 | NT>(rws, vo, WS_QG);
        g1 = ld16<SC1 | NT>(rws, vo + 128, WS_QG);
        if (none(((g0[1] ^ tag) | (g0[3] ^ tag) | (g1[1] ^ tag) | (g1[3] ^ tag)) != 0)) break;
        __builtin_amdgcn_s_sleep(4);
      }
      *(LAS v4u*)(L + L_QF + lane * 16) = v4u{g0[0], g0[2], g1[0], g1[2]};
      lds_arrive(L, L_FQ, lane);
      TS(2);
    }
    if (!isO) {
      while ((int)(NOW() - tl) < GT0) __builtin_amdgcn_s_sleep(4);
      const int t = min(isE ? dq * 9 + min(n16, 8) : 45 + dq * 4 + (n16 & 3), 71) + (G & 1) * 72;
#pragma nounroll
      for (unsigned n = 0; n < SPIN_G; ++n) {
        MEMFENCE();
        if (none(ld4<SC1>(rws, t * 128, WS_GF) != tag)) break;
        __builtin_amdgcn_s_sleep(8);
      }
      MEMFENCE();
      TS(3);
    }
    lds_arrive(L, L_FG, lane);
    if (!hasC) { TSOUT(); return; }
  } else {
  const rsrc_t rws = mk(p.ws, WS_END);
  const bool isR = isE && du < 36;
  const int w = wv - 4;
  const size_t kvoff = ((size_t)((mem >> 1) * HKV + kvh) * SK + (SK - WIN) + (mem & 1) * 512) * (DH * 2) + w * 1024;
  const rsrc_t rk = mk((const char*)p.kc + kvoff, 131072 - w * 1024), rv = mk((const char*)p.vc + kvoff, 131072 - w * 1024);
  const int kvo = (n16 >> 2) * 256 + (G * 4 + (n16 & 3)) * 16, vvo = lane * 16;
  int a_lse = w == 2 && lane < 2 ? WS_LS + (kvh * 16 + mem) * 128 + lane * 8 : WS_END;
  int l_ow = L_OR + w * ORW + (lane & 31) * 4;
  int l_or = L_OR + ((lane & 3) * 72 + (w & 1) * 16 + (lane >> 2)) * 4;
  int a_opub = w < 2 ? WS_OG + (kvh * 16 + mem) * 1024 + w * 512 + lane * 8 : WS_END;
  int a_1 = WS_PS + du * (28 * 512) + (lane >> 3) * 512 + w * 128 + (lane & 7) * 16;
  int a_2 = (lane & 8 ? NB * DM : ((du & 3) * 2 + (lane >> 5)) * DM + (du >> 2) * 64 + w * 16 + (lane & 7) * 2 + ((lane >> 4) & 1)) * 2;
  int l_cw = L_DUMP + lane * 8;
  int l_cnto = lane == 0 ? L_CNTO : l_cw;
  PIN(a_lse); PIN(l_ow); PIN(l_or); PIN(a_opub); PIN(a_1); PIN(a_2); PIN(l_cw); PIN(l_cnto);
  __builtin_amdgcn_s_setprio(2);
  __builtin_amdgcn_s_barrier();
  SBAR();
  TS(1);
  v4u K[32], V[4][8];
  int lag = 0;
  unsigned tb = t0;
#define SLOT_GO(i) do { SBAR(); if (TAU && ((i) % CHK) == 0) { if (SYNC && (i) == SAPP) tb = t0 + __builtin_amdgcn_readfirstlane(lds_ld(L, L_DT)); lag = until(tb + TOFF + ((18 + (i)) * TAU) / 16); } \
    if ((i) >= WW) VMWAIT(WW - 1); if (WLO < WW && (i) >= WLO) vmwait_if_calm<WLO - 1>(lag); SBAR(); } while (0)
#define ISSUE_K(j) do { SLOT_GO(j); K[j] = ld16<NT>(rk, kvo, (j) * 4096); SBAR(); } while (0)
#define ISSUE_V(j) do { SLOT_GO(32 + (j)); V[(j) >> 3][(j) & 7] = ld16<NT>(rv, vvo, (j) * 4096); if ((j) == FVPOS) *(volatile LAS unsigned*)(L + L_FV) = 1u; SBAR(); } while (0)
#pragma unroll
  for (int j = 0; j < 32; ++j) ISSUE_K(j);
#pragma unroll
  for (int j = 0; j < NVQ; ++j) ISSUE_V(j);
  TS(2);
  lds_wait_ge<L_FQ, 1>(1);
  TS(3);
  const v4u qf = *(LAS v4u*)(L + L_QF + lane * 16);
  const unsigned tag = lds_ld(L, L_TAG);
  float s[32];
#pragma unroll
  for (int j = 0; j < 32; j += 4) {
    ISSUE_V(NVQ + (j >> 2));
    f32x4 d[4];
#pragma unroll
    for (int k = 0; k < 4; ++k) d[k] = mfma(K[j + k], qf, f32x4{0.f, 0.f, 0.f, 0.f});
    SBAR();
#pragma unroll
    for (int k = 0; k < 4; ++k) { s[j + k] = dpp<0x55>(d[k][1]) + d[k][0]; PIN(s[j + k]); }
#pragma unroll
    for (int k = 0; k < 4; ++k) { s[j + k] = dpp<0xAA>(d[k][2]) + s[j + k]; PIN(s[j + k]); }
#pragma unroll
    for (int k = 0; k < 4; ++k) { s[j + k] = dpp<0xFF>(d[k][3]) + s[j + k]; PIN(s[j + k]); }
  }
  ISSUE_V(NVQ + 8);
  float m = s[0];
#pragma unroll
  for (int j = 1; j < 32; ++j) m = fmaxf(m, s[j]);
  m = xrow_max(m);
  constexpr float C2 = 0.08838834764831845f * 1.4426950408889634f;
  const float mc = m * C2;
  float l = 0.f;
  v4u pf[4];
#pragma unroll
  for (int j = 0; j < 32; j += 2) {
    if ((j & 7) == 0) ISSUE_V(NVQ + 9 + (j >> 3));
    const float p0 = __builtin_amdgcn_exp2f(fmaf(s[j], C2, -mc)), p1 = __builtin_amdgcn_exp2f(fmaf(s[j + 1], C2, -mc));
    l += p0 + p1;
    pf[j >> 3][(j & 7) >> 1] = cvtpk(p0, p1);
    if ((j & 7) == 6) PIN(pf[j >> 3]);
  }
  ISSUE_V(NVQ + 13);
  l = xrow_sum(l);
  *(LAS f32x2*)(L + ((lane & 59) == 0 ? L_ML + (w * 2 + (lane >> 2)) * 8 : l_cw)) = f32x2{mc, l};
  LGKM0();
  asm volatile("ds_add_u32 %0, %1" ::"v"(lane == 0 ? L_CNTML : l_cw), "v"(1) : "memory");
  TS(4);
  f32x4 acc[8];
#pragma unroll
  for (int j = 0; j < 8; ++j) acc[j] = f32x4{0.f, 0.f, 0.f, 0.f};
  ISSUE_V(NVQ + 14); tile_mma<0, 4>(V[0], pf[0], acc);
  ISSUE_V(NVQ + 15); tile_mma<4, 8>(V[0], pf[0], acc);
#pragma unroll
  for (int j = NVQ + 16; j < 24; ++j) ISSUE_V(j);
  ISSUE_V(24); tile_mma<0, 4>(V[1], pf[1], acc);
  ISSUE_V(25); tile_mma<4, 8>(V[1], pf[1], acc);
  ISSUE_V(26);
  lds_wait_ge<L_CNTML, 1>(4);
  float fw[4], lse;
  {
    f32x2 ml[4];
#pragma unroll
    for (int u = 0; u < 4; ++u) ml[u] = *(LAS f32x2*)(L + L_ML + (u * 2 + ((w == 2 ? lane : w) & 1)) * 8);
    const float M = fmaxf(fmaxf(ml[0][0], ml[1][0]), fmaxf(ml[2][0], ml[3][0]));
    float Ls = 0.f;
#pragma unroll
    for (int u = 0; u < 4; ++u) { fw[u] = __builtin_amdgcn_exp2f(ml[u][0] - M); Ls = fmaf(ml[u][1], fw[u], Ls); }
    const float inv = __builtin_amdgcn_rcpf(Ls);
#pragma unroll
    for (int u = 0; u < 4; ++u) { fw[u] *= inv; PIN(fw[u]); }
    lse = M + __builtin_amdgcn_logf(Ls);
    PIN(lse);
  }
  ISSUE_V(27);
  ISSUE_V(28);
  ISSUE_V(29);
  ISSUE_V(30); tile_mma<0, 4>(V[2], pf[2], acc);
  ISSUE_V(31); tile_mma<4, 8>(V[2], pf[2], acc);
  TS(5);
  v4u bl[8];
#define PERM_V3(mm) _Pragma("unroll") for (int j = 0; j < 8; ++j) bl[j][mm] = (j & 1) ? phi(V[3][2 * (mm) + 1][j >> 1], V[3][2 * (mm)][j >> 1]) : plo(V[3][2 * (mm) + 1][j >> 1], V[3][2 * (mm)][j >> 1]); _Pragma("unroll") for (int j = 0; j < 8; ++j) PIN(bl[j])
  SBAR();
  PERM_V3(0); SBAR(); PERM_V3(1); SBAR();
  if (LSELEFT >= 3) { VMWAIT(LSELEFT); st8<SC1>(v2u{__float_as_uint(lse), tag}, rws, a_lse); SBAR(); }
  PERM_V3(2); SBAR();
  if (LSELEFT < 3) { VMWAIT(LSELEFT); st8<SC1>(v2u{__float_as_uint(lse), tag}, rws, a_lse); SBAR(); }
#pragma unroll
  for (int j = 0; j < 8; ++j) {
    bl[j][3] = (j & 1) ? phi(V[3][7][j >> 1], V[3][6][j >> 1]) : plo(V[3][7][j >> 1], V[3][6][j >> 1]);
    acc[j] = mfma(pf[3], bl[j], acc[j]);
  }
  SBAR();
  TS(6);
#pragma unroll
  for (int j = 0; j < 8; ++j) *(LAS float*)(L + l_ow + j * 144) = acc[j][0];
  asm volatile("ds_add_u32 %0, %1" ::"v"(l_cnto), "v"(1) : "memory");
  lds_wait_ge<L_CNTO, 0>(4);
  {
    f32x2 o = {0.f, 0.f};
#pragma unroll
    for (int u = 0; u < 4; ++u) o += f32x2{*(const LAS float*)(L + l_or + u * ORW), *(const LAS float*)(L + l_or + u * ORW + 144)} * fw[u];
    st8<SC1>(v2u{cvtpk(o[0], o[1]), tag}, rws, a_opub);
  }
  TS(7);
  SBAR();
  if (isR) {
    const f32x2 gt = *(const LAS f32x2*)(L + L_GATE);
    f32x2 ht = {1.f - gt[0], 1.f - gt[1]};
    PIN(ht);
    const unsigned t8 = tag & 0xFFu, t8h = lane < 32 ? t8 : 0u;
    const int a_3 = lane < 32 ? a_1 : WS_END;
    const rsrc_t ro = mk(p.out, NB * DM * 2);
    v4u pg[4];
    unsigned bad = 1;
    if (RPRE) {
#pragma unroll
      for (int i = 0; i < 2; ++i) { pg[i] = ld16<SC1>(rws, a_1, i * 4096); asm volatile("" ::"v"(pg[i])); }
    }
    if (RCLK) until(tb + TOFF + 81 * TAU / 16 + DCLK + RCLK);
    TS(11);
#pragma nounroll
    for (unsigned n = 0; n < SPIN_G; ++n) {
      MEMFENCE();
      if (bad) {
#pragma unroll
        for (int i = 0; i < 3; ++i) pg[i] = ld16<SC1>(rws, a_1, i * 4096);
        pg[3] = ld16<SC1>(rws, a_3, 3 * 4096);
        bad = 0;
#pragma unroll
        for (int i = 0; i < 4; ++i)
#pragma unroll
          for (int d = 0; d < 4; ++d) { pg[i][d] ^= i < 3 ? t8 : t8h; bad |= pg[i][d]; }
        bad &= 0xFFu;
      }
      if (none(bad != 0)) break;
      __builtin_amdgcn_s_sleep(1);
    }
    const f32x4 a4 = __builtin_bit_cast(f32x4, pg[0]) + __builtin_bit_cast(f32x4, pg[1]), b4 = __builtin_bit_cast(f32x4, pg[2]) + __builtin_bit_cast(f32x4, pg[3]);
    const f32x2 r0 = gt * f32x2{a4[0], a4[1]} + ht * f32x2{b4[0], b4[1]}, r1 = gt * f32x2{a4[2], a4[3]} + ht * f32x2{b4[2], b4[3]};
    float res = swap16_sum(swap32_sum(r0[0], r0[1]), swap32_sum(r1[0], r1[1]));
    res += dpp<0x128>(res);
    __builtin_amdgcn_raw_buffer_store_b16((unsigned short)cvtpk(res, res), ro, a_2, 0, OAUX);
    if (du == 0 && w == 0 && lane == 0) st4<SC1>(tag, rws, WS_E);
    TS(10);
  }
  TSOUT();
  return;
  }

  {
    const rsrc_t rws = mk(p.ws, WS_END);
    const int w = wv;
    const int cc = isO ? mem : dc, chunk = isO ? kvh : 21 + dq;
    const char* cb = isO ? (const char*)p.wo + (size_t)(kvh * 256 + w * 64) * (DM * 2) : (const char*)p.wd + (size_t)(2880 + dq * 256 + w * 64) * (DM * 2);
    const rsrc_t rc = mk(cb + cc * 128, isO || dq * 4 + w < 27 ? 63 * DM * 2 + 128 : 0);
    int cvo = G * 16 * (DM * 2) + n16 * 8;
    int a_gu = isD ? WS_GU + min(45 + dq * 4 + w, 71) * 1024 + lane * 16 : WS_END;
    int a_1 = isO && lane < 16 ? WS_LS + (kvh * 16 + ((n16 + 4 * w) & 15)) * 128 : WS_END;
    int a_2 = WS_OG + kvh * 16384 + ((n16 >> 2) & 1) * 8192 + (n16 >> 3) * 1024 + w * 256 + G * 64 + (n16 & 3) * 16;
    int l_cw = L_RC + w * RCW + lane * 8;
    int l_cr = L_RC + (lane & 3) * RCJ + (w & 1) * 512 + (w >> 1) * 128 + (lane >> 2) * 8;
    int a_ppub = WS_PS + (cc * 4 + w) * (28 * 512) + chunk * 512 + lane * 8;
    PIN(cvo); PIN(a_gu); PIN(a_1); PIN(a_2); PIN(l_cw); PIN(l_cr); PIN(a_ppub);
    lds_wait_ge<L_FG, 4>(1);
    lds_wait_ge<L_FV, 2>(1);
    const unsigned tag = lds_ld(L, L_TAG);
    const unsigned tb = t0 + (SYNC ? __builtin_amdgcn_readfirstlane(lds_ld(L, L_DT)) : 0);
    unsigned tc = NOW() + CLEAD;
    { const unsigned ts = tb + TOFF + COFF * TAU / 16; tc = (int)(ts - tc) > 0 ? ts : tc; }
    TS(5);
    v2u c[16];
    v4u lg;
#define ISSUE_C(e) c[e] = ld8<0>(rc, cvo + ((e) & 3) * (DM * 2), ((e) >> 2) * 4 * (DM * 2))
    SBAR();
    const v4u gg = ld16<SC1>(rws, a_gu, 0), uu = ld16<SC1>(rws, a_gu, 72 * 1024);
#pragma unroll
    for (int sl = 0; sl < 8; ++sl) {
      SBAR(); until(tc + sl * CTAU / 16); SBAR();
      ISSUE_C(2 * sl); ISSUE_C(2 * sl + 1);
      if (sl == LGSL && !DCLK) lg = ld16<SC1>(rws, a_1, 0);
      SBAR();
    }
    TS(6);
    f32x2 wt[4];
    v4u a0, a1, bf[8];
#define CFRAGS() _Pragma("unroll") for (int j = 0; j < 8; ++j) { _Pragma("unroll") for (int mm = 0; mm < 4; ++mm) \
      bf[j][mm] = (j & 1) ? phi(c[(j >> 2) * 8 + 2 * mm + 1][(j & 3) >> 1], c[(j >> 2) * 8 + 2 * mm][(j & 3) >> 1]) : plo(c[(j >> 2) * 8 + 2 * mm + 1][(j & 3) >> 1], c[(j >> 2) * 8 + 2 * mm][(j & 3) >> 1]); \
      PIN(bf[j]); }
    if (isO) {
      v4u og[4];
      if (DIRECT) {
        lds_wait_ge<L_CNTO, 0>(4);
        until(NOW() + DIRECT);
        if (DCLK) { until(tb + TOFF + 81 * TAU / 16 + DCLK); SBAR(); lg = ld16<SC1>(rws, a_1, 0); }
        SBAR();
#pragma unroll
        for (int u = 0; u < 4; ++u) og[u] = ld16<SC1>(rws, a_2 + (u & 1) * 2048, (u >> 1) * 4096);
        SBAR();
        CFRAGS();
        SBAR();
      }
      bool lbad = ((lg[1] ^ tag) | (lg[3] ^ tag)) != 0 && lane < 16;
      if (!none(lbad)) {
#pragma nounroll
        for (unsigned n = 0; n < SPIN_G; ++n) {
          MEMFENCE();
          if (lbad) { lg = ld16<SC1>(rws, a_1, 0); lbad = ((lg[1] ^ tag) | (lg[3] ^ tag)) != 0; }
          if (none(lbad)) break;
        }
      }
      TS(11);
      if (!DIRECT) {
#pragma unroll
        for (int u = 0; u < 4; ++u) og[u] = ld16<SC1>(rws, a_2 + (u & 1) * 2048, (u >> 1) * 4096);
      }
      SBAR();
#pragma unroll
      for (int h = 0; h < 2; ++h) {
        const float own = __uint_as_float(lg[2 * h]);
        const float wo_ = __builtin_amdgcn_rcpf(1.f + __builtin_amdgcn_exp2f(dpp<0xB1>(own) - own));
#pragma unroll
        for (int s2 = 0; s2 < 2; ++s2) wt[h * 2 + s2] = f32x2{rdlane(wo_, s2), rdlane(wo_, 2 + s2)};
      }
      if (!DIRECT) { CFRAGS(); }
      SBAR();
#pragma nounroll
      for (unsigned n = 0; n < SPIN_G; ++n) {
        unsigned bad = 0;
#pragma unroll
        for (int u = 0; u < 4; ++u) bad |= (og[u][1] ^ tag) | (og[u][3] ^ tag);
        if (none(bad != 0)) break;
        MEMFENCE();
        if (bad != 0) {
#pragma unroll
          for (int u = 0; u < 4; ++u) og[u] = ld16<SC1>(rws, a_2 + (u & 1) * 2048, (u >> 1) * 4096);
        }
      }
      unsigned x0, x1, x2, x3, x4, x5, x6, x7, y0, y1, y2, y3, y4, y5, y6, y7;
#define CM1 " quad_perm:[1,0,3,2] row_mask:0xf bank_mask:0xf\n\t"
#define CM2 " quad_perm:[2,3,0,1] row_mask:0xf bank_mask:0xf\n\t"
      asm volatile("s_nop 1\n\ts_mov_b64 vcc, %24\n\t"
                   "v_cndmask_b32_dpp %0, %17, %16, vcc" CM1 "v_cndmask_b32_dpp %2, %19, %18, vcc" CM1 "v_cndmask_b32_dpp %4, %21, %20, vcc" CM1 "v_cndmask_b32_dpp %6, %23, %22, vcc" CM1
                   "s_not_b64 vcc, vcc\n\t"
                   "v_cndmask_b32_dpp %1, %16, %17, vcc" CM1 "v_cndmask_b32_dpp %3, %18, %19, vcc" CM1 "v_cndmask_b32_dpp %5, %20, %21, vcc" CM1 "v_cndmask_b32_dpp %7, %22, %23, vcc" CM1
                   "s_mov_b64 vcc, %25\n\t"
                   "v_cndmask_b32_dpp %8, %2, %0, vcc" CM2 "v_cndmask_b32_dpp %9, %6, %4, vcc" CM2 "v_cndmask_b32_dpp %10, %3, %1, vcc" CM2 "v_cndmask_b32_dpp %11, %7, %5, vcc" CM2
                   "s_not_b64 vcc, vcc\n\t"
                   "v_cndmask_b32_dpp %12, %0, %2, vcc" CM2 "v_cndmask_b32_dpp %13, %4, %6, vcc" CM2 "v_cndmask_b32_dpp %14, %1, %3, vcc" CM2 "v_cndmask_b32_dpp %15, %5, %7, vcc" CM2
                   "s_nop 1"
                   : "=&v"(x0), "=&v"(x1), "=&v"(x2), "=&v"(x3), "=&v"(x4), "=&v"(x5), "=&v"(x6), "=&v"(x7),
                     "=&v"(y0), "=&v"(y1), "=&v"(y2), "=&v"(y3), "=&v"(y4), "=&v"(y5), "=&v"(y6), "=&v"(y7)
                   : "v"(og[0][0]), "v"(og[1][0]), "v"(og[2][0]), "v"(og[3][0]), "v"(og[0][2]), "v"(og[1][2]), "v"(og[2][2]), "v"(og[3][2]),
                     "s"(0x5555555555555555ull), "s"(0x3333333333333333ull)
                   : "vcc", "scc");
      a0 = v4u{y0, y1, y2, y3}; a1 = v4u{y4, y5, y6, y7};
    } else {
      const bool up = n16 >= 8;
      const unsigned h0 = hpair(gg[0], uu[0]), h1 = hpair(gg[1], uu[1]), h2 = hpair(gg[2], uu[2]), h3 = hpair(gg[3], uu[3]);
      a0 = v4u{up ? 0u : h0, up ? 0u : h1, up ? h0 : 0u, up ? h1 : 0u};
      a1 = v4u{up ? 0u : h2, up ? 0u : h3, up ? h2 : 0u, up ? h3 : 0u};
      PIN(a0); PIN(a1);
#pragma unroll
      for (int u = 0; u < 4; ++u) wt[u] = f32x2{1.f, 1.f};
      SBAR();
      CFRAGS();
    }
    TS(8);
    f32x4 ca[4];
#pragma unroll
    for (int j = 0; j < 4; ++j) ca[j] = mfma(a0, bf[j], f32x4{0.f, 0.f, 0.f, 0.f});
#pragma unroll
    for (int j = 0; j < 4; ++j) ca[j] = mfma(a1, bf[4 + j], ca[j]);
    SBAR();
#pragma unroll
    for (int j = 0; j < 4; ++j) {
      *(LAS f32x2*)(L + l_cw + j * RCJ) = f32x2{ca[j][0], ca[j][1]};
      *(LAS f32x2*)(L + l_cw + j * RCJ + 512) = f32x2{ca[j][2], ca[j][3]};
    }
    LGKM0();
    __builtin_amdgcn_s_barrier();
    MEMFENCE();
    f32x2 rd[8];
#pragma unroll
    for (int u = 0; u < 8; ++u) rd[u] = *(const LAS f32x2*)(L + l_cr + (u >> 1) * RCW + (u & 1) * 256);
    SBAR();
    f32x2 t0_ = {0.f, 0.f}, t1_ = {0.f, 0.f};
#pragma unroll
    for (int u = 0; u < 4; ++u) { t0_ += rd[2 * u] * wt[(u >> 1) * 2]; t1_ += rd[2 * u + 1] * wt[(u >> 1) * 2 + 1]; }
    t0_ += t1_;
    st8<SC1>(v2u{tagf(t0_[0], tag), tagf(t0_[1], tag)}, rws, a_ppub);
    TS(9);
    TSOUT();
  }
}
}

extern "C" void kernel_launch(void* const* d_in, const int* in_sizes, int n_in, void* d_out, int out_size, void* d_ws, size_t ws_size,
                              hipStream_t stream) {
  Params p;
  p.x = d_in[0]; p.kc = d_in[1]; p.vc = d_in[2]; p.wq = d_in[3]; p.wo = d_in[4];
  p.wg = d_in[6]; p.wu = d_in[7]; p.wd = d_in[8]; p.gw = d_in[9];
  p.out = d_out; p.ws = d_ws;
  fused_kernel<<<256, 512, 0, stream>>>(p);
}
```

```cpp
#include <hip/hip_runtime.h>
#include <hip/hip_bf16.h>
#include <stdint.h>

#define STAMP 0
#define WW 8
#define WLO 5
#define LAG 30
#define WS 11
#define TAU 229
#define TOFF -20
#define CHK 4
#define CTAU TAU
#define COFF 83
#ifndef NVQ
#define NVQ 8
#endif
#define XPOS 5
#define FVPOS 30
#define QT0 570
#define QJ 80
#define GT0 770
#define LSELEFT 2
#define SYNC 1
#define OAUX 0
#define NPRE 2
#define LGPOS 1
#define ST0 370
#define SAPP 24
#define LGSL 5
#define DIRECT 35
#define OPRE 1
#define CWIN 12
#define CLEAD 10
#define DCLK 190
#define RCLK 120
#define RPRE 1
#define TLBW 1
#define CODE_BYTES 18688

namespace {
constexpr int NB = 8, DM = 576, HKV = 16, SK = 4096, DH = 128, WIN = 1024;
typedef unsigned v4u __attribute__((ext_vector_type(4)));
typedef unsigned v2u __attribute__((ext_vector_type(2)));
typedef float f32x4 __attribute__((ext_vector_type(4)));
typedef float f32x2 __attribute__((ext_vector_type(2)));
typedef short bf16x8 __attribute__((ext_vector_type(8)));
typedef __bf16 bf16x2 __attribute__((ext_vector_type(2)));
#define LAS __attribute__((address_space(3)))
#define GAS __attribute__((address_space(1)))
typedef __amdgpu_buffer_rsrc_t rsrc_t;
constexpr int NT = 2, SC1 = 16, RS3 = 0x00020000;

constexpr int WS_E = 0;
constexpr int WS_T0 = 1024;
constexpr int WS_GF = 4096;
constexpr int WS_LS = WS_GF + 144 * 128;
constexpr int WS_QG = WS_LS + 256 * 128;
constexpr int WS_GU = WS_QG + 64 * 2048;
constexpr int WS_OG = WS_GU + 144 * 1024;
constexpr int WS_PS = WS_OG + 256 * 1024;
constexpr int WS_END = WS_PS + 36 * 28 * 512;
constexpr int WS_TS = 8 << 20;

constexpr int L_TAG = 0, L_CNTA = 4, L_CNTML = 8, L_FQ = 12, L_FG = 16, L_FT = 20, L_FV = 24, L_DT = 28, L_CNTO = 32, L_GATE = 40;
constexpr int L_ML = 64;
constexpr int L_QF = 256;
constexpr int ORW = 8 * 36 * 4;
constexpr int L_OR = L_QF + 1024;
constexpr int SLOT = 576 * 4;
constexpr int L_RA = L_OR + 4 * ORW;
constexpr int RCJ = 1024 + 32, RCW = 4 * RCJ;
constexpr int L_RC = L_RA + 6 * SLOT;
constexpr int L_DUMP = L_RC + 4 * RCW;
constexpr int L_END = L_DUMP + 512;

struct Params {
  const void* x; const void* kc; const void* vc; const void* wq; const void* wo; const void* wg; const void* wu; const void* wd; const void* gw;
  void* out; void* ws;
};

__device__ __forceinline__ rsrc_t mk(const void* p, unsigned bytes) { return __builtin_amdgcn_make_buffer_rsrc((void*)p, 0, (int)bytes, RS3); }
template <int AUX> __device__ __forceinline__ v4u ld16(rsrc_t r, int vo, int so) { return __builtin_amdgcn_raw_buffer_load_b128(r, vo, so, AUX); }
template <int AUX> __device__ __forceinline__ v2u ld8(rsrc_t r, int vo, int so) { return __builtin_amdgcn_raw_buffer_load_b64(r, vo, so, AUX); }
template <int AUX> __device__ __forceinline__ unsigned ld4(rsrc_t r, int vo, int so) { return __builtin_amdgcn_raw_buffer_load_b32(r, vo, so, AUX); }
template <int AUX> __device__ __forceinline__ void st16(v4u v, rsrc_t r, int vo) { __builtin_amdgcn_raw_buffer_store_b128(v, r, vo, 0, AUX); }
template <int AUX> __device__ __forceinline__ void st8(v2u v, rsrc_t r, int vo) { __builtin_amdgcn_raw_buffer_store_b64(v, r, vo, 0, AUX); }
template <int AUX> __device__ __forceinline__ void st4(unsigned v, rsrc_t r, int vo) { __builtin_amdgcn_raw_buffer_store_b32(v, r, vo, 0, AUX); }
__device__ __forceinline__ f32x4 mfma(v4u a, v4u b, f32x4 c) {
  return __builtin_amdgcn_mfma_f32_16x16x32_bf16(__builtin_bit_cast(bf16x8, a), __builtin_bit_cast(bf16x8, b), c, 0, 0, 0);
}
__device__ __forceinline__ unsigned cvtpk(float lo, float hi) {
  f32x2 v = {lo, hi};
  return __builtin_bit_cast(unsigned, __builtin_convertvector(v, bf16x2));
}
__device__ __forceinline__ unsigned tagf(float v, unsigned tag) { return __builtin_amdgcn_perm(__float_as_uint(v), tag, 0x07060500u); }
__device__ __forceinline__ float dot2(unsigned a, unsigned b, float c) {
  return __builtin_amdgcn_fdot2_f32_bf16(__builtin_bit_cast(bf16x2, a), __builtin_bit_cast(bf16x2, b), c, false);
}
__device__ __forceinline__ float bflo(unsigned u) { return __builtin_bit_cast(float, u << 16); }
__device__ __forceinline__ float bfhi(unsigned u) { return __builtin_bit_cast(float, u & 0xffff0000u); }
template <int CTRL> __device__ __forceinline__ float dpp(float x) {
  return __builtin_bit_cast(float, __builtin_amdgcn_mov_dpp(__builtin_bit_cast(int, x), CTRL, 0xf, 0xf, true));
}
template <int CTRL> __device__ __forceinline__ unsigned dppu(unsigned x) { return (unsigned)__builtin_amdgcn_mov_dpp((int)x, CTRL, 0xf, 0xf, true); }
__device__ __forceinline__ float rdlane(float x, int l) { return __builtin_bit_cast(float, __builtin_amdgcn_readlane(__builtin_bit_cast(int, x), l)); }
__device__ __forceinline__ float xrow_max(float x) {
  auto s = __builtin_amdgcn_permlane16_swap(__float_as_uint(x), __float_as_uint(x), false, false);
  x = fmaxf(__uint_as_float(s[0]), __uint_as_float(s[1]));
  auto t = __builtin_amdgcn_permlane32_swap(__float_as_uint(x), __float_as_uint(x), false, false);
  return fmaxf(__uint_as_float(t[0]), __uint_as_float(t[1]));
}
__device__ __forceinline__ float swap32_sum(float a, float b) {
  auto t = __builtin_amdgcn_permlane32_swap(__float_as_uint(a), __float_as_uint(b), false, false);
  return __uint_as_float(t[0]) + __uint_as_float(t[1]);
}
__device__ __forceinline__ float swap16_sum(float a, float b) {
  auto t = __builtin_amdgcn_permlane16_swap(__float_as_uint(a), __float_as_uint(b), false, false);
  return __uint_as_float(t[0]) + __uint_as_float(t[1]);
}
__device__ __forceinline__ float xrow_sum(float x) { x = swap16_sum(x, x); return swap32_sum(x, x); }
__device__ __forceinline__ float sigm(float x) { return __builtin_amdgcn_rcpf(1.f + __builtin_amdgcn_exp2f(-1.4426950408889634f * x)); }
__device__ __forceinline__ bool none(bool b) { return __builtin_amdgcn_ballot_w64(b) == 0ull; }
#define SBAR() __builtin_amdgcn_sched_barrier(0)
#define MEMFENCE() asm volatile("" ::: "memory")
#define LGKM0() asm volatile("s_waitcnt lgkmcnt(0)" ::: "memory")
#define PIN(x) asm volatile("" : "+v"(x))
constexpr int vmc(int n) { return (n & 15) | (7 << 4) | (15 << 8) | ((n >> 4) << 14); }
#define VMWAIT(n) __builtin_amdgcn_s_waitcnt(vmc(n))
#define NOW() ((unsigned)__builtin_amdgcn_s_memrealtime())

__device__ __forceinline__ int until(unsigned t) {
  int d;
  asm volatile("1:\n\ts_memrealtime vcc\n\ts_waitcnt lgkmcnt(0)\n\ts_sub_i32 %0, %1, vcc_lo\n\ts_cmp_gt_i32 %0, 0\n\ts_cbranch_scc1 1b" : "=&s"(d) : "s"(t) : "vcc", "scc");
  return d;
}
template <int N> __device__ __forceinline__ void vmwait_if_calm(int d) {
  asm volatile("s_cmp_gt_i32 %0, %1\n\ts_cbranch_scc0 1f\n\ts_waitcnt vmcnt(%2)\n1:" ::"s"(d), "i"(-LAG), "i"(N) : "scc");
}
template <int OFF, int SL> __device__ __forceinline__ void lds_wait_ge(int want) {
  int v, s, n;
  asm volatile("s_mov_b32 %2, 0x400000\n"
               "1:\n\tv_mov_b32 %0, 0\n\tds_read_b32 %0, %0 offset:%4\n\ts_waitcnt lgkmcnt(0)\n\tv_readfirstlane_b32 %1, %0\n\ts_cmp_ge_u32 %1, %3\n\ts_cbranch_scc1 2f\n\t"
               "s_sleep %5\n\ts_sub_u32 %2, %2, 1\n\ts_cbranch_scc0 1b\n2:"
               : "=&v"(v), "=&s"(s), "=&s"(n) : "s"(want), "i"(OFF), "i"(SL) : "scc", "memory");
}
constexpr unsigned SPIN_G = 1u << 16;

__device__ __forceinline__ unsigned lds_ld(LAS unsigned char* L, int off) { return __hip_atomic_load((LAS unsigned*)(L + off), __ATOMIC_RELAXED, __HIP_MEMORY_SCOPE_WORKGROUP); }
__device__ __forceinline__ unsigned lds_arrive(LAS unsigned char* L, int off, int lane) {
  LGKM0();
  unsigned old = 0;
  if (lane == 0) old = __hip_atomic_fetch_add((LAS unsigned*)(L + off), 1u, __ATOMIC_RELAXED, __HIP_MEMORY_SCOPE_WORKGROUP);
  old = __builtin_amdgcn_readfirstlane(old);
  MEMFENCE();
  return old;
}
__device__ __forceinline__ unsigned plo(unsigned hi_row, unsigned lo_row) { return __builtin_amdgcn_perm(hi_row, lo_row, 0x05040100u); }
__device__ __forceinline__ unsigned phi(unsigned hi_row, unsigned lo_row) { return __builtin_amdgcn_perm(hi_row, lo_row, 0x07060302u); }
template <int J0, int J1> __device__ __forceinline__ void tile_mma(const v4u (&r)[8], v4u a, f32x4 (&acc)[8]) {
#pragma unroll
  for (int j = J0; j < J1; ++j) {
    v4u bfr;
#pragma unroll
    for (int m = 0; m < 4; ++m) bfr[m] = (j & 1) ? phi(r[2 * m + 1][j >> 1], r[2 * m][j >> 1]) : plo(r[2 * m + 1][j >> 1], r[2 * m][j >> 1]);
    acc[j] = mfma(a, bfr, acc[j]);
  }
#pragma unroll
  for (int j = J0; j < J1; ++j) PIN(acc[j]);
}
__device__ __forceinline__ unsigned hpair(unsigned gp, unsigned up) {
  const float g0 = bflo(gp), g1 = bfhi(gp);
  return cvtpk(g0 * sigm(g0) * bflo(up), g1 * sigm(g1) * bfhi(up));
}

#define TS(i)
#define TSDECL
#define TSOUT()

typedef unsigned long long u64;
typedef u64 u64x8 __attribute__((ext_vector_type(8)));
typedef u64 u64x2 __attribute__((ext_vector_type(2)));
#define PINS(x) asm volatile("" : "+s"(x))

__global__ void __launch_bounds__(512, 2) fused_kernel(Params unused) {
  __shared__ __attribute__((aligned(16))) unsigned char smem[L_END + (STAMP ? 512 : 0)];
  LAS unsigned char* L = (LAS unsigned char*)smem;
  u64x8 ka; u64x2 kb; u64 kws, pc0;
  asm volatile("s_load_dwordx16 %0, %4, 0x0\n\ts_load_dwordx4 %1, %4, 0x40\n\ts_load_dwordx2 %2, %4, 0x50\n\ts_getpc_b64 %3"
               : "=&s"(ka), "=&s"(kb), "=&s"(kws), "=&s"(pc0) : "s"(__builtin_amdgcn_kernarg_segment_ptr()));
  SBAR();
  const int tid = threadIdx.x, lane = tid & 63, wv = __builtin_amdgcn_readfirstlane(tid >> 6);
  const int wg = blockIdx.x, xcd = wg & 7, ii = wg >> 3, grp = ii >> 4, mem = ii & 15;
  const int kvh = 2 * xcd + grp;
  const int G = lane >> 4, n16 = lane & 15;
  const bool isE = mem >= 13;
  const int du = (mem - (isE ? 13 : 9)) * 16 + kvh;
  const int dq = (du * 57) >> 9, dc = du - 9 * dq;
  const int sa = grp * 13 + mem;
  const bool isQ = !isE && sa < 8;
  const int colblk = xcd + 8 * (sa < 8 ? sa : sa < 17 ? sa - 8 : sa - 17);
  int rs = isE ? DM * 2 : isQ ? 8192 : 9216;
  int vo = (G * (isE ? 16 : 8) + (n16 >> 3) * (isE ? 4 : 32)) * rs + (n16 & 7) * 16;
  int j3 = isE ? 5 * rs : rs, j7 = isE ? 53 * rs : 57 * rs;
  int so = wv * 192 * rs + (isE ? dq * (576 * DM * 2) + dc * 128 : colblk * 128);
  int wsz = isE ? (du >= 45 ? 0 : 4608 * DM * 2) : DM * rs;
  int wsel = isE ? 3 : isQ ? 0 : sa < 17 ? 1 : 2;
  int xvo = (n16 & 7) * (DM * 2) + ((n16 >> 3) * 32 + G * 8) * 2 + wv * 384 + (isE ? NB * DM * 2 : 0);
  PINS(rs); PIN(vo); PINS(j3); PINS(j7); PINS(so); PINS(wsz); PINS(wsel); PIN(xvo);
  SBAR();
  asm volatile("s_waitcnt lgkmcnt(0)" : "+s"(ka), "+s"(kb), "+s"(kws));
  struct { const void *x, *kc, *vc, *wq, *wo, *wg, *wu, *wd, *gw; void *out, *ws; } p = {(const void*)ka[0], (const void*)ka[1], (const void*)ka[2], (const void*)ka[3], (const void*)ka[4],
      (const void*)ka[5], (const void*)ka[6], (const void*)ka[7], (const void*)kb[0], (void*)kb[1], (void*)kws};
  const unsigned t0 = NOW();
  const bool isO = mem < 9, isD = !isO && !isE && du < 63, hasC = isO || isD;
  TSDECL;

  if (__builtin_expect(wv < 3, 1)) {
    const u64 w01 = wsel & 1 ? ka[5] : ka[3], w23 = wsel & 1 ? ka[7] : ka[6];
    const rsrc_t rw = mk((const void*)(wsel & 2 ? w23 : w01), wsz);
    const rsrc_t rx = mk(p.x, NB * DM * 2);
    v4u r[3][8], xa[3];
#pragma unroll
    for (int i = 0; i < 24; ++i) {
      if (TAU && i >= 6 && i % 3 == 0) until(t0 + TOFF + i * TAU * 3 / 64);
      if (i >= WS) { if (i - WS < XPOS + 1) VMWAIT(WS + 2); else VMWAIT(WS - 1); }
      SBAR();
      r[i >> 3][i & 7] = ld16<NT>(rw, vo, so);
      so += (i & 7) == 7 ? j7 : (i & 7) == 3 ? j3 : rs;
      if (i == XPOS) {
#pragma unroll
        for (int u = 0; u < 3; ++u) xa[u] = ld16<0>(rx, xvo + u * 128, 0);
      }
      SBAR();
    }
    __builtin_amdgcn_s_barrier();
    SBAR();
    TS(1);
    const rsrc_t rws = mk(p.ws, WS_END);
    f32x4 acc[8];
#pragma unroll
    for (int j = 0; j < 8; ++j) acc[j] = f32x4{0.f, 0.f, 0.f, 0.f};
    if (!isE) {
#pragma unroll
      for (int t = 0; t < 3; ++t) { tile_mma<0, 8>(r[t], xa[t], acc); SBAR(); }
    } else {
      if (du >= 45) { TSOUT(); return; }
      v4u bfr[3][8];
#pragma unroll
      for (int t = 0; t < 3; ++t)
#pragma unroll
        for (int j = 0; j < 8; ++j) {
#pragma unroll
          for (int m = 0; m < 4; ++m) bfr[t][j][m] = (j & 1) ? phi(r[t][2 * m + 1][j >> 1], r[t][2 * m][j >> 1]) : plo(r[t][2 * m + 1][j >> 1], r[t][2 * m][j >> 1]);
          PIN(bfr[t][j]);
        }
      lds_wait_ge<L_FG, 4>(1);
      lds_wait_ge<L_FV, 4>(1);
      const int gvo = WS_GU + (dq * 9 + wv * 3) * 1024 + lane * 16;
      v4u gg[3], uu[3];
#pragma unroll
      for (int t = 0; t < 3; ++t) { gg[t] = ld16<SC1>(rws, gvo + t * 1024, 0); uu[t] = ld16<SC1>(rws, gvo + t * 1024, 72 * 1024); }
      SBAR();
#pragma unroll
      for (int t = 0; t < 3; ++t) {
        v4u ha;
#pragma unroll
        for (int d = 0; d < 4; ++d) ha[d] = hpair(gg[t][d], uu[t][d]);
#pragma unroll
        for (int j = 0; j < 8; ++j) acc[j] = mfma(ha, bfr[t][j], acc[j]);
      }
    }
    TS(4);
    if ((G >> 1) == (n16 >> 3)) {
      LAS unsigned char* q = L + L_RA + (wv * 2 + (n16 >> 3)) * SLOT + ((G & 1) * 4 * 32 + (n16 & 7) * 4) * 4;
#pragma unroll
      for (int rr = 0; rr < 4; ++rr) {
        *(LAS f32x4*)(q + rr * 128) = f32x4{acc[0][rr], acc[1][rr], acc[2][rr], acc[3][rr]};
        *(LAS f32x4*)(q + rr * 128 + 288 * 4) = f32x4{acc[4][rr], acc[5][rr], acc[6][rr], acc[7][rr]};
      }
    }
    TS(2);
    if (lds_arrive(L, L_CNTA, lane) == 2) {
      lds_wait_ge<L_FT, 1>(1);
      const unsigned tag = lds_ld(L, L_TAG);
      if (isE) {
#pragma unroll
        for (int i = 0; i < 2; ++i) {
          const int c2 = lane & 31, bp = 2 * i + (lane >> 5);
          const LAS unsigned char* q = L + L_RA + (((c2 >> 1) & 1) * 288 + bp * 64 + (c2 >> 2) * 4 + (c2 & 1) * 2) * 4;
          f32x2 s0 = {0.f, 0.f}, s1 = {0.f, 0.f};
#pragma unroll
          for (int sl = 0; sl < 6; ++sl) { s0 += *(const LAS f32x2*)(q + sl * SLOT); s1 += *(const LAS f32x2*)(q + sl * SLOT + 128); }
          st16<SC1>(v4u{tagf(s0[0], tag), tagf(s1[0], tag), tagf(s0[1], tag), tagf(s1[1], tag)}, rws, WS_PS + (dc * 4 + bp) * (28 * 512) + (16 + dq) * 512 + c2 * 16);
        }
      } else {
        const int i8 = lane & 7, rb = isQ ? lane >> 3 : i8, q0 = isQ ? 2 * i8 : 4 * G + (n16 >> 3), q1 = isQ ? q0 + 1 : q0 + 2;
        const LAS unsigned char* p0 = L + L_RA + ((q0 & 1) * 288 + rb * 32 + (q0 >> 1) * 4) * 4;
        const LAS unsigned char* p1 = L + L_RA + ((q1 & 1) * 288 + rb * 32 + (q1 >> 1) * 4) * 4;
        f32x4 s0 = {0.f, 0.f, 0.f, 0.f}, s1 = {0.f, 0.f, 0.f, 0.f};
#pragma unroll
        for (int sl = 0; sl < 6; ++sl) { s0 += *(const LAS f32x4*)(p0 + sl * SLOT); s1 += *(const LAS f32x4*)(p1 + sl * SLOT); }
        const unsigned c0 = cvtpk(s0[0], s0[1]), c1 = cvtpk(s0[2], s0[3]), c2 = cvtpk(s1[0], s1[1]), c3 = cvtpk(s1[2], s1[3]);
        if (isQ) {
          const int o = WS_QG + colblk * 2048 + (lane >> 3) * 256 + i8 * 16;
          st16<SC1 | NT>(v4u{c0, tag, c1, tag}, rws, o);
          st16<SC1 | NT>(v4u{c2, tag, c3, tag}, rws, o + 128);
        } else {
          const int gs = colblk + (sa < 17 ? 0 : 72);
          st16<SC1>(v4u{c0, c1, c2, c3}, rws, WS_GU + gs * 1024 + lane * 16);
          asm volatile("s_waitcnt vmcnt(0)" ::: "memory");
          if (lane < 8) st16<SC1>(v4u{tag, tag, tag, tag}, rws, WS_GF + gs * 128 + lane * 16);
        }
      }
      TS(3);
    }
    if (!hasC) { TSOUT(); return; }
  } else if (wv == 3) {
    const rsrc_t rws = mk(p.ws, WS_END);
    const bool isR = isE && du < 36;
    const unsigned e0 = ld4<SC1>(rws, 0, WS_E);
    if (SYNC && ii == 0 && lane == 0) st4<SC1>(t0, rws, WS_T0 + xcd * 4);
    const v4u cp = ld16<0>(mk((const void*)(pc0 & ~127ull), CODE_BYTES), lane * 16, ii * 1024);
    unsigned tw = 0;
    if (TLBW) {
      const size_t kvo_ = ((size_t)((mem >> 1) * HKV + kvh) * SK + (SK - WIN) + (mem & 1) * 512) * (DH * 2);
      const char* cb_ = isO ? (const char*)p.wo + (size_t)kvh * (256 * DM * 2) : (const char*)p.wd + (size_t)(2880 + (isD ? dq : 0) * 256) * (DM * 2);
      const char* a_ = lane == 0 ? (const char*)p.kc + kvo_ : lane == 1 ? (const char*)p.vc + kvo_ : lane == 2 ? cb_ : lane == 3 ? cb_ + 191 * (DM * 2) : (const char*)p.out + (du & 31) * 128;
      if (lane < (isR ? 5 : 4)) tw = __builtin_nontemporal_load((const GAS unsigned*)a_);
    }
    if (lane < 8) *(LAS unsigned*)(L + 4 + lane * 4) = 0u;
    LGKM0();
    SBAR();
    __builtin_amdgcn_s_barrier();
    SBAR();
    const unsigned tag = __builtin_amdgcn_readfirstlane(e0) + 1u;
    if (lane == 0) *(LAS unsigned*)(L + L_TAG) = tag;
    lds_arrive(L, L_FT, lane);
    asm volatile("" ::"v"(cp), "v"(tw));
    TS(1);
    if (isR) {
      const int rb = (du & 3) * 2 + (lane >> 5), l32 = lane & 31;
      const rsrc_t rx = mk(p.x, NB * DM * 2), rg = mk(p.gw, DM * 2);
      float gs = 0.f;
      v4u xv[3], gv[3];
#pragma unroll
      for (int u = 0; u < 3; ++u) {
        xv[u] = ld16<0>(rx, (u == 2 && l32 >= 8 ? NB : rb) * (DM * 2) + l32 * 16 + u * 512, 0);
        gv[u] = ld16<0>(rg, l32 * 16 + u * 512, 0);
      }
#pragma unroll
      for (int u = 0; u < 3; ++u)
#pragma unroll
        for (int d = 0; d < 4; ++d) gs = dot2(xv[u][d], gv[u][d], gs);
      gs += dpp<0xB1>(gs); gs += dpp<0x4E>(gs); gs += dpp<0x141>(gs); gs += dpp<0x128>(gs);
      gs = swap16_sum(gs, gs);
      if (l32 == 0) *(LAS float*)(L + L_GATE + (lane >> 5) * 4) = sigm(gs);
    }
    if (isO) {
      const unsigned z = ld4<SC1>(rws, lane < 16 ? WS_LS + (kvh * 16 + lane) * 128 : OPRE && lane < 32 && mem < 8 ? WS_OG + kvh * 16384 + mem * 2048 + (lane - 16) * 128 : WS_END, 0);
      asm volatile("" ::"v"(z));
    }
    unsigned tl = t0;
    if (SYNC) {
      while ((int)(NOW() - t0) < ST0) __builtin_amdgcn_s_sleep(2);
      int d = (int)(ld4<SC1>(rws, (lane & 7) * 4, WS_T0) - t0);
      d = (unsigned)(d + 256) < 512u ? d : 0;
      int sm = d, mx = d;
      sm += (int)dppu<0xB1>(sm); sm += (int)dppu<0x4E>(sm); sm += (int)dppu<0x141>(sm);
      mx = max(mx, (int)dppu<0xB1>(mx)); mx = max(mx, (int)dppu<0x4E>(mx)); mx = max(mx, (int)dppu<0x141>(mx));
      if (lane == 0) *(volatile LAS unsigned*)(L + L_DT) = (t0 + (sm >> 3)) | 1u;
      tl = t0 + __builtin_amdgcn_readfirstlane(mx);
    }
    while ((int)(NOW() - tl) < QT0 + (((wg >> 4) * 5 + xcd * 2) & 15) * QJ / 16) __builtin_amdgcn_s_sleep(2);
    {
      const int c16 = 4 * G + (n16 & 3), h = (n16 >> 2) & 1;
      const int vo = (kvh * 4 + 2 * h + (c16 >> 3)) * 2048 + (mem >> 1) * 256 + (c16 & 7) * 16;
      v4u g0, g1;
#pragma nounroll
      for (unsigned n = 0; n < SPIN_G; ++n) {
        MEMFENCE();
        g0 = ld16<SC1 | NT>(rws, vo, WS_QG);
        g1 = ld16<SC1 | NT>(rws, vo + 128, WS_QG);
        if (none(((g0[1] ^ tag) | (g0[3] ^ tag) | (g1[1] ^ tag) | (g1[3] ^ tag)) != 0)) break;
        __builtin_amdgcn_s_sleep(4);
      }
      *(LAS v4u*)(L + L_QF + lane * 16) = v4u{g0[0], g0[2], g1[0], g1[2]};
      lds_arrive(L, L_FQ, lane);
      TS(2);
    }
    if (!isO) {
      while ((int)(NOW() - tl) < GT0) __builtin_amdgcn_s_sleep(4);
      const int t = min(isE ? dq * 9 + min(n16, 8) : 45 + dq * 4 + (n16 & 3), 71) + (G & 1) * 72;
#pragma nounroll
      for (unsigned n = 0; n < SPIN_G; ++n) {
        MEMFENCE();
        if (none(ld4<SC1>(rws, t * 128, WS_GF) != tag)) break;
        __builtin_amdgcn_s_sleep(8);
      }
      MEMFENCE();
      TS(3);
    }
    lds_arrive(L, L_FG, lane);
    if (!hasC) { TSOUT(); return; }
  } else {
  const rsrc_t rws = mk(p.ws, WS_END);
  const bool isR = isE && du < 36;
  const int w = wv - 4;
  const size_t kvoff = ((size_t)((mem >> 1) * HKV + kvh) * SK + (SK - WIN) + (mem & 1) * 512) * (DH * 2) + w * 1024;
  const rsrc_t rk = mk((const char*)p.kc + kvoff, 131072 - w * 1024), rv = mk((const char*)p.vc + kvoff, 131072 - w * 1024);
  const int kvo = (n16 >> 2) * 256 + (G * 4 + (n16 & 3)) * 16, vvo = lane * 16;
  int a_lse = w == 2 && lane < 2 ? WS_LS + (kvh * 16 + mem) * 128 + lane * 8 : WS_END;
  int l_ow = L_OR + w * ORW + (lane & 31) * 4;
  int l_or = L_OR + ((lane & 3) * 72 + (w & 1) * 16 + (lane >> 2)) * 4;
  int a_opub = w < 2 ? WS_OG + (kvh * 16 + mem) * 1024 + w * 512 + lane * 8 : WS_END;
  int a_1 = WS_PS + du * (28 * 512) + (lane >> 3) * 512 + w * 128 + (lane & 7) * 16;
  int a_2 = (lane & 8 ? NB * DM : ((du & 3) * 2 + (lane >> 5)) * DM + (du >> 2) * 64 + w * 16 + (lane & 7) * 2 + ((lane >> 4) & 1)) * 2;
  int l_cw = L_DUMP + lane * 8;
  int l_cnto = lane == 0 ? L_CNTO : l_cw;
  PIN(a_lse); PIN(l_ow); PIN(l_or); PIN(a_opub); PIN(a_1); PIN(a_2); PIN(l_cw); PIN(l_cnto);
  __builtin_amdgcn_s_setprio(2);
  __builtin_amdgcn_s_barrier();
  SBAR();
  TS(1);
  v4u K[32], V[4][8];
  int lag = 0;
  unsigned tb = t0;
#define SLOT_GO(i) do { SBAR(); if (TAU && ((i) % CHK) == 0) { if (SYNC && (i) == SAPP) { const unsigned tm_ = __builtin_amdgcn_readfirstlane(lds_ld(L, L_DT)); tb = tm_ ? tm_ : t0; } lag = until(tb + TOFF + ((18 + (i)) * TAU) / 16); } \
    if ((i) >= WW) VMWAIT(WW - 1); if (WLO < WW && (i) >= WLO) vmwait_if_calm<WLO - 1>(lag); SBAR(); } while (0)
#define ISSUE_K(j) do { SLOT_GO(j); K[j] = ld16<NT>(rk, kvo, (j) * 4096); SBAR(); } while (0)
#define ISSUE_V(j) do { SLOT_GO(32 + (j)); V[(j) >> 3][(j) & 7] = ld16<NT>(rv, vvo, (j) * 4096); if ((j) == FVPOS) *(volatile LAS unsigned*)(L + L_FV) = 1u; SBAR(); } while (0)
#pragma unroll
  for (int j = 0; j < 32; ++j) ISSUE_K(j);
#pragma unroll
  for (int j = 0; j < NVQ; ++j) ISSUE_V(j);
  TS(2);
  lds_wait_ge<L_FQ, 1>(1);
  TS(3);
  const v4u qf = *(LAS v4u*)(L + L_QF + lane * 16);
  const unsigned tag = lds_ld(L, L_TAG);
  float s[32];
#pragma unroll
  for (int j = 0; j < 32; j += 4) {
    ISSUE_V(NVQ + (j >> 2));
    f32x4 d[4];
#pragma unroll
    for (int k = 0; k < 4; ++k) d[k] = mfma(K[j + k], qf, f32x4{0.f, 0.f, 0.f, 0.f});
    SBAR();
#pragma unroll
    for (int k = 0; k < 4; ++k) { s[j + k] = dpp<0x55>(d[k][1]) + d[k][0]; PIN(s[j + k]); }
#pragma unroll
    for (int k = 0; k < 4; ++k) { s[j + k] = dpp<0xAA>(d[k][2]) + s[j + k]; PIN(s[j + k]); }
#pragma unroll
    for (int k = 0; k < 4; ++k) { s[j + k] = dpp<0xFF>(d[k][3]) + s[j + k]; PIN(s[j + k]); }
  }
  ISSUE_V(NVQ + 8);
  float m = s[0];
#pragma unroll
  for (int j = 1; j < 32; ++j) m = fmaxf(m, s[j]);
  m = xrow_max(m);
  constexpr float C2 = 0.08838834764831845f * 1.4426950408889634f;
  const float mc = m * C2;
  float l = 0.f;
  v4u pf[4];
#pragma unroll
  for (int j = 0; j < 32; j += 2) {
    if ((j & 7) == 0) ISSUE_V(NVQ + 9 + (j >> 3));
    const float p0 = __builtin_amdgcn_exp2f(fmaf(s[j], C2, -mc)), p1 = __builtin_amdgcn_exp2f(fmaf(s[j + 1], C2, -mc));
    l += p0 + p1;
    pf[j >> 3][(j & 7) >> 1] = cvtpk(p0, p1);
    if ((j & 7) == 6) PIN(pf[j >> 3]);
  }
  ISSUE_V(NVQ + 13);
  l = xrow_sum(l);
  *(LAS f32x2*)(L + ((lane & 59) == 0 ? L_ML + (w * 2 + (lane >> 2)) * 8 : l_cw)) = f32x2{mc, l};
  LGKM0();
  asm volatile("ds_add_u32 %0, %1" ::"v"(lane == 0 ? L_CNTML : l_cw), "v"(1) : "memory");
  TS(4);
  f32x4 acc[8];
#pragma unroll
  for (int j = 0; j < 8; ++j) acc[j] = f32x4{0.f, 0.f, 0.f, 0.f};
  ISSUE_V(NVQ + 14); tile_mma<0, 4>(V[0], pf[0], acc);
  ISSUE_V(NVQ + 15); tile_mma<4, 8>(V[0], pf[0], acc);
#pragma unroll
  for (int j = NVQ + 16; j < 24; ++j) ISSUE_V(j);
  ISSUE_V(24); tile_mma<0, 4>(V[1], pf[1], acc);
  ISSUE_V(25); tile_mma<4, 8>(V[1], pf[1], acc);
  ISSUE_V(26);
  lds_wait_ge<L_CNTML, 1>(4);
  float fw[4], lse;
  {
    f32x2 ml[4];
#pragma unroll
    for (int u = 0; u < 4; ++u) ml[u] = *(LAS f32x2*)(L + L_ML + (u * 2 + ((w == 2 ? lane : w) & 1)) * 8);
    const float M = fmaxf(fmaxf(ml[0][0], ml[1][0]), fmaxf(ml[2][0], ml[3][0]));
    float Ls = 0.f;
#pragma unroll
    for (int u = 0; u < 4; ++u) { fw[u] = __builtin_amdgcn_exp2f(ml[u][0] - M); Ls = fmaf(ml[u][1], fw[u], Ls); }
    const float inv = __builtin_amdgcn_rcpf(Ls);
#pragma unroll
    for (int u = 0; u < 4; ++u) { fw[u] *= inv; PIN(fw[u]); }
    lse = M + __builtin_amdgcn_logf(Ls);
    PIN(lse);
  }
  ISSUE_V(27);
  ISSUE_V(28);
  ISSUE_V(29);
  ISSUE_V(30); tile_mma<0, 4>(V[2], pf[2], acc);
  ISSUE_V(31); tile_mma<4, 8>(V[2], pf[2], acc);
  TS(5);
  v4u bl[8];
#define PERM_V3(mm) _Pragma("unroll") for (int j = 0; j < 8; ++j) bl[j][mm] = (j & 1) ? phi(V[3][2 * (mm) + 1][j >> 1], V[3][2 * (mm)][j >> 1]) : plo(V[3][2 * (mm) + 1][j >> 1], V[3][2 * (mm)][j >> 1]); _Pragma("unroll") for (int j = 0; j < 8; ++j) PIN(bl[j])
  SBAR();
  PERM_V3(0); SBAR(); PERM_V3(1); SBAR();
  if (LSELEFT >= 3) { VMWAIT(LSELEFT); st8<SC1>(v2u{__float_as_uint(lse), tag}, rws, a_lse); SBAR(); }
  PERM_V3(2); SBAR();
  if (LSELEFT < 3) { VMWAIT(LSELEFT); st8<SC1>(v2u{__float_as_uint(lse), tag}, rws, a_lse); SBAR(); }
#pragma unroll
  for (int j = 0; j < 8; ++j) {
    bl[j][3] = (j & 1) ? phi(V[3][7][j >> 1], V[3][6][j >> 1]) : plo(V[3][7][j >> 1], V[3][6][j >> 1]);
    acc[j] = mfma(pf[3], bl[j], acc[j]);
  }
  SBAR();
  TS(6);
#pragma unroll
  for (int j = 0; j < 8; ++j) *(LAS float*)(L + l_ow + j * 144) = acc[j][0];
  asm volatile("ds_add_u32 %0, %1" ::"v"(l_cnto), "v"(1) : "memory");
  lds_wait_ge<L_CNTO, 0>(4);
  {
    f32x2 o = {0.f, 0.f};
#pragma unroll
    for (int u = 0; u < 4; ++u) o += f32x2{*(const LAS float*)(L + l_or + u * ORW), *(const LAS float*)(L + l_or + u * ORW + 144)} * fw[u];
    st8<SC1>(v2u{cvtpk(o[0], o[1]), tag}, rws, a_opub);
  }
  TS(7);
  SBAR();
  if (isR) {
    const f32x2 gt = *(const LAS f32x2*)(L + L_GATE);
    f32x2 ht = {1.f - gt[0], 1.f - gt[1]};
    PIN(ht);
    const unsigned t8 = tag & 0xFFu, t8h = lane < 32 ? t8 : 0u;
    const int a_3 = lane < 32 ? a_1 : WS_END;
    const rsrc_t ro = mk(p.out, NB * DM * 2);
    v4u pg[4];
    unsigned bad = 1;
    if (RPRE) {
#pragma unroll
      for (int i = 0; i < 2; ++i) { pg[i] = ld16<SC1>(rws, a_1, i * 4096); asm volatile("" ::"v"(pg[i])); }
    }
    if (RCLK) until(tb + TOFF + 81 * TAU / 16 + DCLK + RCLK);
    TS(11);
#pragma nounroll
    for (unsigned n = 0; n < SPIN_G; ++n) {
      MEMFENCE();
      if (bad) {
#pragma unroll
        for (int i = 0; i < 3; ++i) pg[i] = ld16<SC1>(rws, a_1, i * 4096);
        pg[3] = ld16<SC1>(rws, a_3, 3 * 4096);
        bad = 0;
#pragma unroll
        for (int i = 0; i < 4; ++i)
#pragma unroll
          for (int d = 0; d < 4; ++d) { pg[i][d] ^= i < 3 ? t8 : t8h; bad |= pg[i][d]; }
        bad &= 0xFFu;
      }
      if (none(bad != 0)) break;
      __builtin_amdgcn_s_sleep(1);
    }
    const f32x4 a4 = __builtin_bit_cast(f32x4, pg[0]) + __builtin_bit_cast(f32x4, pg[1]), b4 = __builtin_bit_cast(f32x4, pg[2]) + __builtin_bit_cast(f32x4, pg[3]);
    const f32x2 r0 = gt * f32x2{a4[0], a4[1]} + ht * f32x2{b4[0], b4[1]}, r1 = gt * f32x2{a4[2], a4[3]} + ht * f32x2{b4[2], b4[3]};
    float res = swap16_sum(swap32_sum(r0[0], r0[1]), swap32_sum(r1[0], r1[1]));
    res += dpp<0x128>(res);
    __builtin_amdgcn_raw_buffer_store_b16((unsigned short)cvtpk(res, res), ro, a_2, 0, OAUX);
    if (du == 0 && w == 0 && lane == 0) st4<SC1>(tag, rws, WS_E);
    TS(10);
  }
  TSOUT();
  return;
  }

  {
    const rsrc_t rws = mk(p.ws, WS_END);
    const int w = wv;
    const int cc = isO ? mem : dc, chunk = isO ? kvh : 21 + dq;
    const char* cb = isO ? (const char*)p.wo + (size_t)(kvh * 256 + w * 64) * (DM * 2) : (const char*)p.wd + (size_t)(2880 + dq * 256 + w * 64) * (DM * 2);
    const rsrc_t rc = mk(cb + cc * 128, isO || dq * 4 + w < 27 ? 63 * DM * 2 + 128 : 0);
    int cvo = G * 16 * (DM * 2) + n16 * 8;
    int a_gu = isD ? WS_GU + min(45 + dq * 4 + w, 71) * 1024 + lane * 16 : WS_END;
    int a_1 = isO && lane < 16 ? WS_LS + (kvh * 16 + ((n16 + 4 * w) & 15)) * 128 : WS_END;
    int a_2 = WS_OG + kvh * 16384 + ((n16 >> 2) & 1) * 8192 + (n16 >> 3) * 1024 + w * 256 + G * 64 + (n16 & 3) * 16;
    int l_cw = L_RC + w * RCW + lane * 8;
    int l_cr = L_RC + (lane & 3) * RCJ + (w & 1) * 512 + (w >> 1) * 128 + (lane >> 2) * 8;
    int a_ppub = WS_PS + (cc * 4 + w) * (28 * 512) + chunk * 512 + lane * 8;
    PIN(cvo); PIN(a_gu); PIN(a_1); PIN(a_2); PIN(l_cw); PIN(l_cr); PIN(a_ppub);
    lds_wait_ge<L_FG, 4>(1);
    lds_wait_ge<L_FV, 2>(1);
    const unsigned tag = lds_ld(L, L_TAG);
    const unsigned tb = SYNC ? __builtin_amdgcn_readfirstlane(lds_ld(L, L_DT)) : t0;
    unsigned tc = NOW() + CLEAD;
    { const unsigned ts = tb + TOFF + COFF * TAU / 16; tc = (int)(ts - tc) > 0 ? ts : tc; }
    TS(5);
    v2u c[16];
    v4u lg;
#define ISSUE_C(e) c[e] = ld8<NT>(rc, cvo + ((e) & 3) * (DM * 2), ((e) >> 2) * 4 * (DM * 2))
    SBAR();
    const v4u gg = ld16<SC1>(rws, a_gu, 0), uu = ld16<SC1>(rws, a_gu, 72 * 1024);
#pragma unroll
    for (int sl = 0; sl < 8; ++sl) {
      SBAR(); until(tc + sl * CTAU / 16); SBAR();
      ISSUE_C(2 * sl); ISSUE_C(2 * sl + 1);
      if (sl == LGSL && !DCLK) lg = ld16<SC1>(rws, a_1, 0);
      SBAR();
    }
    TS(6);
    f32x2 wt[4];
    v4u a0, a1, bf[8];
#define CFRAGS() _Pragma("unroll") for (int j = 0; j < 8; ++j) { _Pragma("unroll") for (int mm = 0; mm < 4; ++mm) \
      bf[j][mm] = (j & 1) ? phi(c[(j >> 2) * 8 + 2 * mm + 1][(j & 3) >> 1], c[(j >> 2) * 8 + 2 * mm][(j & 3) >> 1]) : plo(c[(j >> 2) * 8 + 2 * mm + 1][(j & 3) >> 1], c[(j >> 2) * 8 + 2 * mm][(j & 3) >> 1]); \
      PIN(bf[j]); }
    if (isO) {
      v4u og[4];
      if (DIRECT) {
        lds_wait_ge<L_CNTO, 0>(4);
        until(NOW() + DIRECT);
        if (DCLK) { until(tb + TOFF + 81 * TAU / 16 + DCLK); SBAR(); lg = ld16<SC1>(rws, a_1, 0); }
        SBAR();
#pragma unroll
        for (int u = 0; u < 4; ++u) og[u] = ld16<SC1>(rws, a_2 + (u & 1) * 2048, (u >> 1) * 4096);
        SBAR();
        CFRAGS();
        SBAR();
      }
      bool lbad = ((lg[1] ^ tag) | (lg[3] ^ tag)) != 0 && lane < 16;
      if (!none(lbad)) {
#pragma nounroll
        for (unsigned n = 0; n < SPIN_G; ++n) {
          MEMFENCE();
          if (lbad) { lg = ld16<SC1>(rws, a_1, 0); lbad = ((lg[1] ^ tag) | (lg[3] ^ tag)) != 0; }
          if (none(lbad)) break;
        }
      }
      TS(11);
      if (!DIRECT) {
#pragma unroll
        for (int u = 0; u < 4; ++u) og[u] = ld16<SC1>(rws, a_2 + (u & 1) * 2048, (u >> 1) * 4096);
      }
      SBAR();
#pragma unroll
      for (int h = 0; h < 2; ++h) {
        const float own = __uint_as_float(lg[2 * h]);
        const float wo_ = __builtin_amdgcn_rcpf(1.f + __builtin_amdgcn_exp2f(dpp<0xB1>(own) - own));
#pragma unroll
        for (int s2 = 0; s2 < 2; ++s2) wt[h * 2 + s2] = f32x2{rdlane(wo_, s2), rdlane(wo_, 2 + s2)};
      }
      if (!DIRECT) { CFRAGS(); }
      SBAR();
#pragma nounroll
      for (unsigned n = 0; n < SPIN_G; ++n) {
        unsigned bad = 0;
#pragma unroll
        for (int u = 0; u < 4; ++u) bad |= (og[u][1] ^ tag) | (og[u][3] ^ tag);
        if (none(bad != 0)) break;
        MEMFENCE();
        if (bad != 0) {
#pragma unroll
          for (int u = 0; u < 4; ++u) og[u] = ld16<SC1>(rws, a_2 + (u & 1) * 2048, (u >> 1) * 4096);
        }
      }
      unsigned x0, x1, x2, x3, x4, x5, x6, x7, y0, y1, y2, y3, y4, y5, y6, y7;
#define CM1 " quad_perm:[1,0,3,2] row_mask:0xf bank_mask:0xf\n\t"
#define CM2 " quad_perm:[2,3,0,1] row_mask:0xf bank_mask:0xf\n\t"
      asm volatile("s_nop 1\n\ts_mov_b64 vcc, %24\n\t"
                   "v_cndmask_b32_dpp %0, %17, %16, vcc" CM1 "v_cndmask_b32_dpp %2, %19, %18, vcc" CM1 "v_cndmask_b32_dpp %4, %21, %20, vcc" CM1 "v_cndmask_b32_dpp %6, %23, %22, vcc" CM1
                   "s_not_b64 vcc, vcc\n\t"
                   "v_cndmask_b32_dpp %1, %16, %17, vcc" CM1 "v_cndmask_b32_dpp %3, %18, %19, vcc" CM1 "v_cndmask_b32_dpp %5, %20, %21, vcc" CM1 "v_cndmask_b32_dpp %7, %22, %23, vcc" CM1
                   "s_mov_b64 vcc, %25\n\t"
                   "v_cndmask_b32_dpp %8, %2, %0, vcc" CM2 "v_cndmask_b32_dpp %9, %6, %4, vcc" CM2 "v_cndmask_b32_dpp %10, %3, %1, vcc" CM2 "v_cndmask_b32_dpp %11, %7, %5, vcc" CM2
                   "s_not_b64 vcc, vcc\n\t"
                   "v_cndmask_b32_dpp %12, %0, %2, vcc" CM2 "v_cndmask_b32_dpp %13, %4, %6, vcc" CM2 "v_cndmask_b32_dpp %14, %1, %3, vcc" CM2 "v_cndmask_b32_dpp %15, %5, %7, vcc" CM2
                   "s_nop 1"
                   : "=&v"(x0), "=&v"(x1), "=&v"(x2), "=&v"(x3), "=&v"(x4), "=&v"(x5), "=&v"(x6), "=&v"(x7),
                     "=&v"(y0), "=&v"(y1), "=&v"(y2), "=&v"(y3), "=&v"(y4), "=&v"(y5), "=&v"(y6), "=&v"(y7)
                   : "v"(og[0][0]), "v"(og[1][0]), "v"(og[2][0]), "v"(og[3][0]), "v"(og[0][2]), "v"(og[1][2]), "v"(og[2][2]), "v"(og[3][2]),
                     "s"(0x5555555555555555ull), "s"(0x3333333333333333ull)
                   : "vcc", "scc");
      a0 = v4u{y0, y1, y2, y3}; a1 = v4u{y4, y5, y6, y7};
    } else {
      const bool up = n16 >= 8;
      const unsigned h0 = hpair(gg[0], uu[0]), h1 = hpair(gg[1], uu[1]), h2 = hpair(gg[2], uu[2]), h3 = hpair(gg[3], uu[3]);
      a0 = v4u{up ? 0u : h0, up ? 0u : h1, up ? h0 : 0u, up ? h1 : 0u};
      a1 = v4u{up ? 0u : h2, up ? 0u : h3, up ? h2 : 0u, up ? h3 : 0u};
      PIN(a0); PIN(a1);
#pragma unroll
      for (int u = 0; u < 4; ++u) wt[u] = f32x2{1.f, 1.f};
      SBAR();
      CFRAGS();
    }
    TS(8);
    f32x4 ca[4];
#pragma unroll
    for (int j = 0; j < 4; ++j) ca[j] = mfma(a0, bf[j], f32x4{0.f, 0.f, 0.f, 0.f});
#pragma unroll
    for (int j = 0; j < 4; ++j) ca[j] = mfma(a1, bf[4 + j], ca[j]);
    SBAR();
#pragma unroll
    for (int j = 0; j < 4; ++j) {
      *(LAS f32x2*)(L + l_cw + j * RCJ) = f32x2{ca[j][0], ca[j][1]};
      *(LAS f32x2*)(L + l_cw + j * RCJ + 512) = f32x2{ca[j][2], ca[j][3]};
    }
    LGKM0();
    __builtin_amdgcn_s_barrier();
    MEMFENCE();
    f32x2 rd[8];
#pragma unroll
    for (int u = 0; u < 8; ++u) rd[u] = *(const LAS f32x2*)(L + l_cr + (u >> 1) * RCW + (u & 1) * 256);
    SBAR();
    f32x2 t0_ = {0.f, 0.f}, t1_ = {0.f, 0.f};
#pragma unroll
    for (int u = 0; u < 4; ++u) { t0_ += rd[2 * u] * wt[(u >> 1) * 2]; t1_ += rd[2 * u + 1] * wt[(u >> 1) * 2 + 1]; }
    t0_ += t1_;
    st8<SC1>(v2u{tagf(t0_[0], tag), tagf(t0_[1], tag)}, rws, a_ppub);
    TS(9);
    TSOUT();
  }
}
}

extern "C" void kernel_launch(void* const* d_in, const int* in_sizes, int n_in, void* d_out, int out_size, void* d_ws, size_t ws_size,
                              hipStream_t stream) {
  Params p;
  p.x = d_in[0]; p.kc = d_in[1]; p.vc = d_in[2]; p.wq = d_in[3]; p.wo = d_in[4];
  p.wg = d_in[6]; p.wu = d_in[7]; p.wd = d_in[8]; p.gw = d_in[9];
  p.out = d_out; p.ws = d_ws;
  fused_kernel<<<256, 512, 0, stream>>>(p);
}
```
